```python
import torch

def _softmax(x, axis=-1):
    return torch.softmax(x, dim=axis)

def _gelu(x):
    return torch.nn.functional.gelu(x, approximate='tanh')

def _silu(x):
    return torch.nn.functional.silu(x)

def _sigmoid(x):
    return torch.sigmoid(x)

def _layernorm(x, axis=-1, eps=1e-05):
    m = x.mean(dim=axis, keepdim=True)
    v = x.var(dim=axis, keepdim=True, unbiased=False)
    return (x - m) / torch.sqrt(v + eps)

def _scatter_add(tgt, idx, src, axis=0):
    return tgt.clone().index_add_(axis, idx, src)

def _segment_sum(src, seg, n):
    out = torch.zeros((n,) + src.shape[1:], dtype=src.dtype)
    return out.index_add_(0, seg, src)

def _pad(x, pw):
    flat = tuple((v for pair in reversed(pw) for v in pair))
    return torch.nn.functional.pad(x, flat)
kernel_name = 'adaLN_cond__ssm_conv_h3__glu_gated_reglu__prompt'

def setup_inputs():

    _w = lambda a: (lambda t: t.to(torch.bfloat16) if t.is_floating_point() else t)(torch.as_tensor(a))

    def _A_setup():
        B, S, D = (1, 2048, 576)
        ks = 4
        return {'q': _w((lambda *s: torch.randn(*s, dtype=torch.bfloat16) * 0.05)(B, S, D)), 'k': _w((lambda *s: torch.randn(*s, dtype=torch.bfloat16) * 0.8)(B, S, D)), 'v': _w((lambda *s: torch.randn(*s, dtype=torch.bfloat16) * 0.8)(B, S, D)), 'shift_k': _w((lambda *s: torch.randn(*s, dtype=torch.bfloat16) * 0.8)(D, ks) * 0.5), 'ssm_k': _w((lambda *s: torch.randn(*s, dtype=torch.bfloat16) * 0.8)(D, S) * (1.0 / S ** 0.5)), 'ssm_D': _w((lambda *s: torch.randn(*s, dtype=torch.bfloat16) * 0.8)(D))}
    _A_in = _A_setup()

    def _B_setup():
        B, S, D, F = (1, 2048, 576, 4608)
        return {'x': _w((lambda *s: torch.randn(*s, dtype=torch.bfloat16) * 0.05)(B, S, D)), 'W_gate': _w((lambda *s: torch.randn(*s, dtype=torch.bfloat16) * 0.8)(D, F) * (2.0 / D) ** 0.5), 'W_up': _w((lambda *s: torch.randn(*s, dtype=torch.bfloat16) * 0.8)(D, F) * (2.0 / D) ** 0.5), 'W_down': _w((lambda *s: torch.randn(*s, dtype=torch.bfloat16) * 0.8)(F, D) * (1.0 / F) ** 0.5)}
    _B_in = _B_setup()
    return {'A_q': _A_in['q'], 'A_k': _A_in['k'], 'A_v': _A_in['v'], 'A_shift_k': _A_in['shift_k'], 'A_ssm_k': _A_in['ssm_k'], 'A_ssm_D': _A_in['ssm_D'], 'B_x': _B_in['x'], 'B_W_gate': _B_in['W_gate'], 'B_W_up': _B_in['W_up'], 'B_W_down': _B_in['W_down'], 'c': _w((lambda *s: torch.randn(*s, dtype=torch.bfloat16) * 0.2)(1, 576) * 8.0), 'w_mod': _w((lambda *s: torch.randn(*s, dtype=torch.bfloat16) * 0.2)(576, 6 * 576) * 576 ** (-0.5) * 8.0)}

def reference(A_q, A_k, A_v, A_shift_k, A_ssm_k, A_ssm_D, B_x, B_W_gate, B_W_up, B_W_down, c, w_mod):

    def _A_ref(q, k, v, shift_k, ssm_k, ssm_D):
        B, S, D = q.shape
        ks = shift_k.shape[-1]
        k_pad = _pad(k, ((0, 0), (ks - 1, 0), (0, 0)))
        k_shift = torch.zeros_like(k)
        for t in range(ks):
            k_shift = k_shift + k_pad[:, t:t + S, :] * shift_k[:, ks - 1 - t][None, None, :]
        kv = k_shift * v
        L = 2 * S
        kv_pad = _pad(kv, ((0, 0), (0, S), (0, 0)))
        kf_pad = _pad(ssm_k, ((0, 0), (0, S)))
        kv_f = (lambda x, axes=(-2, -1): torch.fft.rfft2(x.float(), dim=axes))(kv_pad[:, None, :, :], axes=(1, 2))
        kf_f = (lambda x, axes=(-2, -1): torch.fft.rfft2(x.float(), dim=axes))(kf_pad[:, None, :], axes=(1, 2))
        kf_f = (lambda x, *d: x.permute(*d))(kf_f, 1, 2, 0)
        y_f = kv_f * kf_f[None, :, :, :]
        y = (lambda x, s, axes=(-2, -1): torch.fft.irfft2(x, s=s, dim=axes).to(torch.bfloat16))(y_f, (1, L), axes=(1, 2))[:, 0, :S, :]
        y = y + kv * ssm_D[None, None, :]
        return q * y

    def _B_ref(x, W_gate, W_up, W_down):
        gate = torch.einsum('bsd,df->bsf', x, W_gate)
        up = torch.einsum('bsd,df->bsf', x, W_up)
        gate = gate * (lambda x, dt: x.to(dt))(gate > 0, gate.dtype)
        h = gate * up
        return torch.einsum('bsf,fd->bsd', h, W_down)
    x = A_q
    mod = (_silu(c) @ w_mod).reshape(c.shape[0], 6, 1, -1)
    sa, ca, ga, sb, cb, gb = (mod[:, 0].clone(), mod[:, 1].clone(), mod[:, 2].clone(), mod[:, 3].clone(), mod[:, 4].clone(), mod[:, 5].clone())
    h = _layernorm(x, axis=-1) * (1 + ca) + sa
    x = x + ga * _A_ref(h, A_k, A_v, A_shift_k, A_ssm_k, A_ssm_D)
    h = _layernorm(x, axis=-1) * (1 + cb) + sb
    x = x + gb * _B_ref(h, B_W_gate, B_W_up, B_W_down)
    return x
```

```cpp
#include <hip/hip_runtime.h>
#include <hip/hip_bf16.h>
#include <stdint.h>

typedef unsigned short u16;
typedef __attribute__((ext_vector_type(2))) __bf16 bf16x2;
typedef __attribute__((ext_vector_type(8))) __bf16 bf16x8;
typedef __attribute__((ext_vector_type(2))) float f32x2;
typedef __attribute__((ext_vector_type(4))) float f32x4;
typedef __attribute__((ext_vector_type(16))) float f32x16;
typedef __attribute__((ext_vector_type(4))) unsigned u32x4;
typedef __attribute__((ext_vector_type(2))) unsigned u32x2;
typedef __attribute__((ext_vector_type(4))) short s16x4;
#define LDSP __attribute__((address_space(3)))
#define GLBP __attribute__((address_space(1)))

constexpr int S_ = 2048, D_ = 576, F_ = 4608;

constexpr size_t OFF_CNT = 0;
constexpr size_t OFF_MOD = 8192;
constexpr size_t OFF_ST1 = OFF_MOD + 16384;
constexpr size_t OFF_CC  = OFF_ST1 + 8192;
constexpr size_t OFF_UT  = OFF_CC + 32 * 6912;
constexpr size_t OFF_XT  = OFF_UT + (size_t)D_ * S_ * 2;
constexpr size_t OFF_XF  = OFF_XT + (size_t)D_ * S_ * 2;
constexpr size_t OFF_W1  = OFF_XF + (size_t)D_ * S_ * 2;
constexpr size_t OFF_W2  = OFF_W1 + (size_t)D_ * F_ * 4;
constexpr size_t OFF_H2  = OFF_W2 + (size_t)D_ * F_ * 2;
constexpr size_t OFF_PART = OFF_H2 + (size_t)S_ * F_ * 2;

__device__ __forceinline__ unsigned pk2(float a, float b) {
  f32x2 v = {a, b};
  return __builtin_bit_cast(unsigned, __builtin_convertvector(v, bf16x2));
}
__device__ __forceinline__ float lo16(unsigned u) { return __uint_as_float(u << 16); }
__device__ __forceinline__ float hi16(unsigned u) { return __uint_as_float(u & 0xffff0000u); }
__device__ __forceinline__ float bfr(float a) { return lo16(pk2(a, a)); }
__device__ __forceinline__ void bfr2(float& a, float& b) { unsigned u = pk2(a, b); a = lo16(u); b = hi16(u); }
__device__ __forceinline__ float bf2f(u16 x) { return __uint_as_float(((unsigned)x) << 16); }
__device__ __forceinline__ s16x4 tr_read(const char* p) {
  return __builtin_amdgcn_ds_read_tr16_b64_v4i16((LDSP s16x4*)p);
}
__device__ __forceinline__ u32x4 tr_read2(const char* p0, const char* p1) {
  s16x4 a = tr_read(p0), b = tr_read(p1);
  u32x2 x = __builtin_bit_cast(u32x2, a), y = __builtin_bit_cast(u32x2, b);
  u32x4 r = {x[0], x[1], y[0], y[1]};
  return r;
}

constexpr int K0_MOD = 108, K0_KV = 288, K0_ST = 64, K0_WD = 144;
constexpr int K0_GRID = K0_MOD + K0_KV + K0_ST + K0_WD;

__global__ __launch_bounds__(256) void k0_prep(const u16* __restrict__ xq, const u16* __restrict__ kk,
                                               const u16* __restrict__ vv, const u16* __restrict__ shift_k,
                                               const u16* __restrict__ cvec, const u16* __restrict__ w_mod,
                                               const u16* __restrict__ Wd, char* __restrict__ ws) {
  __shared__ __attribute__((aligned(16))) char sm[37888];
  const int tid = threadIdx.x, lane = tid & 63, wave = tid >> 6;
  int bid = blockIdx.x;
  if (bid < K0_MOD) {
    if (bid == 0 && tid < 64) *(unsigned*)(ws + OFF_CNT + tid * 128) = 0u;
    const int c4 = tid & 3, rg = tid >> 2;
    u32x4 w[9];
    float sc[9];
#pragma unroll
    for (int i = 0; i < 9; ++i) {
      const int row = rg + 64 * i;
      w[i] = *(const u32x4*)(w_mod + (size_t)row * 3456 + bid * 32 + c4 * 8);
      sc[i] = bf2f(cvec[row]);
    }
    float acc[8];
#pragma unroll
    for (int e = 0; e < 8; ++e) acc[e] = 0.f;
#pragma unroll
    for (int i = 0; i < 9; ++i) {
      const float s = bfr(sc[i] / (1.f + __expf(-sc[i])));
#pragma unroll
      for (int e = 0; e < 4; ++e) {
        acc[2 * e] += s * lo16(w[i][e]);
        acc[2 * e + 1] += s * hi16(w[i][e]);
      }
    }
#pragma unroll
    for (int e = 0; e < 8; ++e) {
#pragma unroll
      for (int o = 4; o < 64; o <<= 1) acc[e] += __shfl_xor(acc[e], o);
    }
    float* red = (float*)sm;
    if (lane < 4) {
#pragma unroll
      for (int e = 0; e < 8; ++e) red[wave * 32 + lane * 8 + e] = acc[e];
    }
    __syncthreads();
    if (tid < 32) {
      const float r = (red[tid] + red[32 + tid]) + (red[64 + tid] + red[96 + tid]);
      ((float*)(ws + OFF_MOD))[bid * 32 + tid] = bfr(r);
    }
    return;
  }
  bid -= K0_MOD;
  if (bid < K0_KV) {
    const int cb = bid % 9, tb = bid / 9;
    const int c8 = tid & 7, rp = tid >> 3;
    const int r0 = tb * 64 + rp * 2;
    const int ch0 = cb * 64 + c8 * 8;
    u32x4 kr[5], vr[2], xr[2], sw[4];
#pragma unroll
    for (int i = 0; i < 5; ++i) {
      const int r = r0 - 3 + i;
      const int rc = r < 0 ? 0 : r;
      kr[i] = *(const u32x4*)(kk + (size_t)rc * D_ + ch0);
      if (r < 0) kr[i] = (u32x4){0u, 0u, 0u, 0u};
    }
#pragma unroll
    for (int i = 0; i < 2; ++i) {
      vr[i] = *(const u32x4*)(vv + (size_t)(r0 + i) * D_ + ch0);
      xr[i] = *(const u32x4*)(xq + (size_t)(r0 + i) * D_ + ch0);
    }
#pragma unroll
    for (int i = 0; i < 4; ++i) sw[i] = *(const u32x4*)(shift_k + (size_t)ch0 * 4 + i * 8);
    char* tu = sm;
    char* tx = sm + 8192;
    const int q = rp >> 2, inner = rp & 3;
#pragma unroll
    for (int e = 0; e < 8; ++e) {
      const unsigned wa = sw[e >> 1][(e & 1) * 2], wb = sw[e >> 1][(e & 1) * 2 + 1];
      const float w0 = lo16(wa), w1 = hi16(wa), w2 = lo16(wb), w3 = hi16(wb);
      float kf[5];
#pragma unroll
      for (int i = 0; i < 5; ++i) kf[i] = (e & 1) ? hi16(kr[i][e >> 1]) : lo16(kr[i][e >> 1]);
      const float v0 = (e & 1) ? hi16(vr[0][e >> 1]) : lo16(vr[0][e >> 1]);
      const float v1 = (e & 1) ? hi16(vr[1][e >> 1]) : lo16(vr[1][e >> 1]);
      float a0 = kf[0] * w3, a1 = kf[1] * w3; bfr2(a0, a1);
      float t0 = kf[1] * w2, t1 = kf[2] * w2; bfr2(t0, t1);
      a0 += t0; a1 += t1; bfr2(a0, a1);
      t0 = kf[2] * w1; t1 = kf[3] * w1; bfr2(t0, t1);
      a0 += t0; a1 += t1; bfr2(a0, a1);
      t0 = kf[3] * w0; t1 = kf[4] * w0; bfr2(t0, t1);
      a0 += t0; a1 += t1; bfr2(a0, a1);
      const unsigned kvp = pk2(a0 * v0, a1 * v1);
      const unsigned x0 = xr[0][e >> 1], x1 = xr[1][e >> 1];
      const unsigned xp = (e & 1) ? ((x0 >> 16) | (x1 & 0xffff0000u)) : ((x0 & 0xffffu) | (x1 << 16));
      const int off = (c8 * 8 + e) * 128 + ((q ^ c8) * 16) + inner * 4;
      *(unsigned*)(tu + off) = kvp;
      *(unsigned*)(tx + off) = xp;
    }
    __syncthreads();
#pragma unroll
    for (int i = 0; i < 2; ++i) {
      const int p = tid + 256 * i, ch = p >> 3, qq = p & 7;
      const int off = ch * 128 + ((qq ^ (ch >> 3)) * 16);
      const size_t g = ((size_t)(cb * 64 + ch) * S_ + tb * 64 + qq * 8) * 2;
      *(u32x4*)(ws + OFF_UT + g) = *(const u32x4*)(tu + off);
      *(u32x4*)(ws + OFF_XT + g) = *(const u32x4*)(tx + off);
    }
    return;
  }
  bid -= K0_KV;
  if (bid < K0_ST) {
    const int row = bid * 32 + wave * 8 + (lane >> 3), c = lane & 7;
    u32x4 xr[9];
#pragma unroll
    for (int i = 0; i < 9; ++i) xr[i] = *(const u32x4*)(xq + (size_t)row * D_ + (c + 8 * i) * 8);
    float s = 0.f;
#pragma unroll
    for (int i = 0; i < 9; ++i)
#pragma unroll
      for (int e = 0; e < 4; ++e) s += lo16(xr[i][e]) + hi16(xr[i][e]);
    s += __shfl_xor(s, 1); s += __shfl_xor(s, 2); s += __shfl_xor(s, 4);
    const float mean = s * (1.f / 576.f);
    float ss = 0.f;
#pragma unroll
    for (int i = 0; i < 9; ++i)
#pragma unroll
      for (int e = 0; e < 4; ++e) {
        const float a = lo16(xr[i][e]) - mean, b = hi16(xr[i][e]) - mean;
        ss += a * a + b * b;
      }
    ss += __shfl_xor(ss, 1); ss += __shfl_xor(ss, 2); ss += __shfl_xor(ss, 4);
    const float var = bfr(ss * (1.f / 576.f));
    const float sd = sqrtf(bfr(var + 1e-5f));
    if (c == 0) ((unsigned*)(ws + OFF_ST1))[row] = pk2(mean, sd);
    return;
  }
  bid -= K0_ST;
  {
    const int T = bid;
    const char* src = (const char*)Wd + (size_t)T * 32 * 1152;
    u32x4 w[9];
#pragma unroll
    for (int i = 0; i < 9; ++i) w[i] = *(const u32x4*)(src + (tid + 256 * i) * 16);
#pragma unroll
    for (int i = 0; i < 9; ++i) {
      const int p = tid + 256 * i, row = p / 72, cc = p % 72;
      *(u32x4*)(sm + row * 1184 + cc * 16) = w[i];
    }
    __syncthreads();
    const int g = lane >> 4, q = (lane & 15) >> 2, p = lane & 3;
    const char* base = sm + (16 * (g >> 1) + 4 * (g & 1) + q) * 1184 + (wave * 144 + 4 * p) * 2;
    char* dst = ws + OFF_W2 + ((size_t)(((T / 36) * 4 + wave) * 36 + (T % 36)) * 9) * 1024 + lane * 16;
#pragma unroll
    for (int i = 0; i < 9; ++i) {
      const u32x4 r = tr_read2(base + i * 32, base + i * 32 + 8 * 1184);
      *(u32x4*)(dst + i * 1024) = r;
    }
  }
}

constexpr int K1_CONV = 144, K1_W = 108;
constexpr int KC_STRIDE = 4416;
constexpr int CH_BYTES = 4 * KC_STRIDE + 4672;
constexpr int K1_XCH = 4 * CH_BYTES;
constexpr int K1_X1 = K1_XCH + 32768;
constexpr int K1_LDS = K1_X1 + 16384;

__global__ __launch_bounds__(512, 2) void k1_conv(const u16* __restrict__ ssm_k, const u16* __restrict__ ssm_D,
                                                  const u16* __restrict__ Wg, const u16* __restrict__ Wu,
                                                  char* __restrict__ ws) {
  extern __shared__ __attribute__((aligned(16))) char sm[];
  const int tid = threadIdx.x, lane = tid & 63, wave = tid >> 6;
  const float* mod = (const float*)(ws + OFF_MOD);
  int bid = blockIdx.x;
  if (bid < K1_CONV) {
    const int cg = (bid & 7) * 18 + (bid >> 3);
    const int cl = wave >> 1, w2 = wave & 1;
    const int d = cg * 4 + cl;
    char* chb = sm + cl * CH_BYTES;
    char* ub = chb + 4 * KC_STRIDE;
    const int t128 = tid & 127;
    const char* krow = (const char*)ssm_k + (size_t)d * 4096;
    const char* urow = ws + OFF_UT + (size_t)d * 4096;
    const char* xrow = ws + OFF_XT + (size_t)d * 4096;
    u32x4 ka[2], ua[2];
    u32x2 kb[2];
#pragma unroll
    for (int i = 0; i < 2; ++i) {
      const int c = t128 + 128 * i;
      ka[i] = *(const u32x4*)(krow + c * 16);
      const int cn = c < 255 ? c + 1 : 255;
      kb[i] = *(const u32x2*)(krow + cn * 16);
      if (c == 255) kb[i] = (u32x2){0u, 0u};
      ua[i] = *(const u32x4*)(urow + c * 16);
    }
    const int li = lane & 15, g = lane >> 4;
    u32x2 xv[4];
    u32x4 st[4];
#pragma unroll
    for (int i = 0; i < 4; ++i) {
      const int s = 256 * (4 * w2 + i) + 16 * li + 4 * g;
      xv[i] = *(const u32x2*)(xrow + s * 2);
      st[i] = *(const u32x4*)(ws + OFF_ST1 + s * 4);
    }
    const float sa = mod[d], c1a = bfr(1.f + mod[576 + d]), ga = mod[1152 + d];
    const float Dd = bf2f(ssm_D[d]);
    {
      const u32x4 z = {0u, 0u, 0u, 0u};
      if (t128 < 32) *(u32x4*)(ub + t128 * 16) = z;
      else if (t128 < 32 + 12) {
        const int i = t128 - 32;
        *(u32x4*)(chb + (i / 3) * KC_STRIDE + (i % 3) * 16) = z;
      } else if (t128 < 32 + 12 + 16) {
        const int i = t128 - 44;
        *(u32x4*)(chb + (i >> 2) * KC_STRIDE + 64 + 4096 + (i & 3) * 16) = z;
      } else if (t128 < 64) {
        *(u32x4*)(ub + 4608 + (t128 - 60) * 16) = z;
      }
    }
#pragma unroll
    for (int i = 0; i < 2; ++i) {
      const int c = t128 + 128 * i;
      const unsigned w0 = ka[i][0], w1 = ka[i][1], w2_ = ka[i][2], w3 = ka[i][3], w4 = kb[i][0], w5 = kb[i][1];
      const unsigned a0 = __builtin_amdgcn_alignbit(w1, w0, 16), a1 = __builtin_amdgcn_alignbit(w2_, w1, 16),
                     a2 = __builtin_amdgcn_alignbit(w3, w2_, 16), a3 = __builtin_amdgcn_alignbit(w4, w3, 16),
                     a4 = __builtin_amdgcn_alignbit(w5, w4, 16);
      char* p = chb + 64 + c * 16;
      *(u32x4*)(p) = ka[i];
      *(u32x4*)(p + KC_STRIDE) = (u32x4){a0, a1, a2, a3};
      *(u32x4*)(p + 2 * KC_STRIDE) = (u32x4){w1, w2_, w3, w4};
      *(u32x4*)(p + 3 * KC_STRIDE) = (u32x4){a1, a2, a3, a4};
      *(u32x4*)(ub + 512 + c * 16) = ua[i];
      if (c == 0) {
        *(u32x4*)(p - 16) = (u32x4){0u, 0u, 0u, 0u};
        *(u32x4*)(p - 16 + KC_STRIDE) = (u32x4){0u, 0u, 0u, w0 << 16};
        *(u32x4*)(p - 16 + 2 * KC_STRIDE) = (u32x4){0u, 0u, 0u, w0};
        *(u32x4*)(p - 16 + 3 * KC_STRIDE) = (u32x4){0u, 0u, w0 << 16, a0};
      }
    }
    __syncthreads();
    const int q = li >> 2, p4 = li & 3;
    const char* abase = chb + ((4 - q) & 3) * KC_STRIDE + 2 * (32 - 8 * g - (q ? 4 : 0) + 4 * p4) - 8;
    const char* bbase = ub + 2 * (256 + 16 * li + 8 * g);
    f32x4 acc[8];
#pragma unroll
    for (int b = 0; b < 8; ++b) acc[b] = (f32x4){0.f, 0.f, 0.f, 0.f};
#pragma unroll 1
    for (int it = 0; it < 4; ++it) {
      const int r = 2 * it + 1 + w2;
      const char* ap = abase + 64 * r;
      const char* bp = bbase - 64 * r;
      bf16x8 A[8], B[8];
#pragma unroll
      for (int i = 0; i < 8; ++i) {
        A[i] = __builtin_bit_cast(bf16x8, tr_read2(ap + 512 * i + 8, ap + 512 * i));
        B[i] = *(const bf16x8*)(bp + 512 * i);
      }
#pragma unroll
      for (int b = 0; b < 8; ++b)
#pragma unroll
        for (int qq = 0; qq <= b; ++qq)
          acc[b] = __builtin_amdgcn_mfma_f32_16x16x32_bf16(A[qq], B[b - qq], acc[b], 0, 0, 0);
    }
    char* xch = sm + K1_XCH;
    f32x4 fin[4];
    if (w2 == 0) {
#pragma unroll
      for (int i = 0; i < 4; ++i) { *(f32x4*)(xch + wave * 4096 + i * 1024 + lane * 16) = acc[4 + i]; fin[i] = acc[i]; }
    } else {
#pragma unroll
      for (int i = 0; i < 4; ++i) { *(f32x4*)(xch + wave * 4096 + i * 1024 + lane * 16) = acc[i]; fin[i] = acc[4 + i]; }
    }
    {
      const bf16x8 A0 = __builtin_bit_cast(bf16x8, tr_read2(abase + 8, abase));
#pragma unroll
      for (int i = 0; i < 4; ++i) {
        const bf16x8 Bd = *(const bf16x8*)(bbase + 512 * (4 * w2 + i));
        fin[i] = __builtin_amdgcn_mfma_f32_16x16x32_bf16(A0, Bd, fin[i], 0, 0, 0);
      }
    }
    __syncthreads();
    char* x1s = sm + K1_X1;
#pragma unroll
    for (int i = 0; i < 4; ++i) {
      const f32x4 o = *(const f32x4*)(xch + (wave ^ 1) * 4096 + i * 1024 + lane * 16);
      const int s = 256 * (4 * w2 + i) + 16 * li + 4 * g;
      const u32x2 uu = *(const u32x2*)(ub + 512 + s * 2);
#pragma unroll
      for (int e = 0; e < 2; ++e) {
        const float x0 = lo16(xv[i][e]), x1 = hi16(xv[i][e]);
        const unsigned s0 = st[i][2 * e], s1 = st[i][2 * e + 1];
        float h0 = x0 - lo16(s0), h1 = x1 - lo16(s1); bfr2(h0, h1);
        h0 = h0 * __builtin_amdgcn_rcpf(hi16(s0)); h1 = h1 * __builtin_amdgcn_rcpf(hi16(s1)); bfr2(h0, h1);
        h0 *= c1a; h1 *= c1a; bfr2(h0, h1);
        h0 += sa; h1 += sa; bfr2(h0, h1);
        float y0 = fin[i][2 * e] + o[2 * e], y1 = fin[i][2 * e + 1] + o[2 * e + 1]; bfr2(y0, y1);
        float t0 = lo16(uu[e]) * Dd, t1 = hi16(uu[e]) * Dd; bfr2(t0, t1);
        y0 += t0; y1 += t1; bfr2(y0, y1);
        y0 *= h0; y1 *= h1; bfr2(y0, y1);
        y0 *= ga; y1 *= ga; bfr2(y0, y1);
        const unsigned r = pk2(x0 + y0, x1 + y1);
        *(u16*)(x1s + (s + 2 * e) * 8 + cl * 2) = (u16)(r & 0xffffu);
        *(u16*)(x1s + (s + 2 * e + 1) * 8 + cl * 2) = (u16)(r >> 16);
      }
    }
    __syncthreads();
    const int d0 = cg * 4;
    char* xf = ws + OFF_XF + ((size_t)(d0 >> 4) * 64 + ((d0 >> 3) & 1) * 32) * 16 + ((d0 >> 2) & 1) * 8;
#pragma unroll
    for (int i = 0; i < 4; ++i) {
      const int s = tid + 512 * i;
      *(u32x2*)(xf + (size_t)(s >> 5) * 36864 + (s & 31) * 16) = *(const u32x2*)(x1s + s * 8);
    }
    return;
  }
  bid -= K1_CONV;
  {
    const int cbk = bid % 36, kt = bid / 36;
    const int mat = cbk / 18, cblk = cbk % 18;
    const u16* W = mat ? Wu : Wg;
    const int p = tid & 31, rg = tid >> 5;
    u32x4 w[12];
    float cs[12], sb[12];
#pragma unroll
    for (int i = 0; i < 12; ++i) {
      const int k = kt * 192 + rg + 16 * i;
      w[i] = *(const u32x4*)(W + (size_t)k * F_ + cblk * 256 + p * 8);
      cs[i] = mod[4 * 576 + k];
      sb[i] = mod[3 * 576 + k];
    }
    float a1[8], a2[8];
#pragma unroll
    for (int e = 0; e < 8; ++e) { a1[e] = 0.f; a2[e] = 0.f; }
#pragma unroll
    for (int i = 0; i < 12; ++i) {
      const float c1 = bfr(1.f + cs[i]);
      u32x4 o;
#pragma unroll
      for (int e = 0; e < 4; ++e) {
        const float l = lo16(w[i][e]), h = hi16(w[i][e]);
        const unsigned r = pk2(l * c1, h * c1);
        o[e] = r;
        a1[2 * e] += lo16(r); a1[2 * e + 1] += hi16(r);
        a2[2 * e] += sb[i] * l; a2[2 * e + 1] += sb[i] * h;
      }
      *(u32x4*)(sm + (rg + 16 * i) * 576 + p * 16) = o;
    }
    float* red = (float*)(sm + 110592);
#pragma unroll
    for (int e = 0; e < 8; ++e) { a1[e] += __shfl_xor(a1[e], 32); a2[e] += __shfl_xor(a2[e], 32); }
    if (lane < 32) {
      *(f32x4*)(red + wave * 512 + p * 8) = (f32x4){a1[0], a1[1], a1[2], a1[3]};
      *(f32x4*)(red + wave * 512 + p * 8 + 4) = (f32x4){a1[4], a1[5], a1[6], a1[7]};
    } else {
      *(f32x4*)(red + wave * 512 + 256 + p * 8) = (f32x4){a2[0], a2[1], a2[2], a2[3]};
      *(f32x4*)(red + wave * 512 + 256 + p * 8 + 4) = (f32x4){a2[4], a2[5], a2[6], a2[7]};
    }
    __syncthreads();
    {
      float r = 0.f;
#pragma unroll
      for (int i = 0; i < 8; ++i) r += red[i * 512 + tid];
      const int which = tid >> 8, col = tid & 255;
      const int ft = cblk * 16 + (col >> 4);
      ((float*)(ws + OFF_CC))[(ft / 9) * 1728 + kt * 576 + which * 288 + (ft % 9) * 32 + mat * 16 + (col & 15)] = r;
    }
    const int gm = lane >> 4, q = (lane & 15) >> 2, p4 = lane & 3;
    const int ftl = wave * 2 + (gm & 1), h = gm >> 1;
    const int ft = cblk * 16 + ftl;
    const char* base = sm + (8 * h + q) * 576 + (ftl * 16 + 4 * p4) * 2;
    char* dst = ws + OFF_W1 + ((size_t)(ft / 3) * 108 + (size_t)kt * 36 + (ft % 3)) * 1024 + (h * 32 + mat * 16 + (lane & 15)) * 16;
#pragma unroll
    for (int ks = 0; ks < 12; ++ks) {
      const u32x4 r = tr_read2(base + ks * 16 * 576, base + ks * 16 * 576 + 4 * 576);
      *(u32x4*)(dst + ks * 3072) = r;
    }
  }
}

constexpr int UNIT = 36864;

__device__ __forceinline__ void dma_unit(const char* gbase  , unsigned voff, unsigned lds_dst  ) {
  unsigned keep;
  asm volatile(
      "s_mov_b32 %0, m0\n\t"
      "s_mov_b32 m0, %3\n\t"
      "s_mov_b32 exec_hi, 0\n\t"
      "global_load_lds_dwordx4 %1, %2\n\t"
      "s_mov_b32 exec_hi, -1\n\t"
      "global_load_lds_dwordx4 %1, %2 offset:512\n\t"
      "global_load_lds_dwordx4 %1, %2 offset:1536\n\t"
      "global_load_lds_dwordx4 %1, %2 offset:2560\n\t"
      "global_load_lds_dwordx4 %1, %2 offset:3584\n\t"
      "s_mov_b32 m0, %0"
      : "=&s"(keep)
      : "v"(voff), "s"(gbase), "s"(lds_dst)
      : "memory");
}
template <int OFF>
__device__ __forceinline__ void ld_frag(u32x4& dst, const char* gbase  , unsigned voff) {
  asm volatile("global_load_dwordx4 %0, %1, %2 offset:%3" : "=v"(dst) : "v"(voff), "s"(gbase), "i"(OFF) : "memory");
}
#define WAIT_VM(n) asm volatile("s_waitcnt vmcnt(" #n ")" ::: "memory")
#define PIN4(a, b, c, d) asm volatile("" : "+v"(a), "+v"(b), "+v"(c), "+v"(d))

constexpr int K3_CCRAW = 4 * UNIT;
constexpr int K3_CC = K3_CCRAW + 6912;
constexpr int K3_LDS = K3_CC + 2304;

template <int U>
__device__ __forceinline__ void k3_unit(const char* sm, int lane, const u32x4 (&X)[36], f32x16 (&acc)[3]) {
  const char* slot = sm + (U & 3) * UNIT + lane * 16;
  constexpr int KB = (U % 3) * 12;
#pragma unroll
  for (int ks = 0; ks < 12; ++ks) {
#pragma unroll
    for (int ti = 0; ti < 3; ++ti) {
      const bf16x8 a = *(const bf16x8*)(slot + (ks * 3 + ti) * 1024);
      acc[ti] = __builtin_amdgcn_mfma_f32_32x32x16_bf16(a, __builtin_bit_cast(bf16x8, X[KB + ks]), acc[ti], 0, 0, 0);
    }
  }
}

__device__ __forceinline__ void k3_epi(const char* sm, int lane, int grp, float m2, float rsd, f32x16 (&acc)[3],
                                       char* hdst, int ft0) {
  const int h = lane >> 5;
#pragma unroll
  for (int ti = 0; ti < 3; ++ti) {
    const char* cp = sm + K3_CC + ((grp * 3 + ti) * 32 + 4 * h) * 4;
    float val[16];
#pragma unroll
    for (int qq = 0; qq < 4; ++qq) {
      const f32x4 c1 = *(const f32x4*)(cp + qq * 32);
      const f32x4 c2 = *(const f32x4*)(cp + 1152 + qq * 32);
#pragma unroll
      for (int r = 0; r < 4; ++r) val[qq * 4 + r] = (acc[ti][qq * 4 + r] - m2 * c1[r]) * rsd + c2[r];
    }
    u32x4 o;
#pragma unroll
    for (int e = 0; e < 4; ++e) {
      const unsigned gp = pk2(val[2 * e], val[2 * e + 1]);
      const unsigned up = pk2(val[8 + 2 * e], val[8 + 2 * e + 1]);
      o[e] = pk2(fmaxf(lo16(gp), 0.f) * lo16(up), fmaxf(hi16(gp), 0.f) * hi16(up));
    }
    const int ft = ft0 + grp * 3 + ti;
    *(u32x4*)(hdst + (size_t)(ft >> 1) * 1024 + (ft & 1) * 512) = o;
#pragma unroll
    for (int r = 0; r < 16; ++r) acc[ti][r] = 0.f;
  }
}

__global__ __launch_bounds__(512, 2) void k3_gemm1(char* __restrict__ ws) {
  extern __shared__ __attribute__((aligned(16))) char sm[];
  const int tid = threadIdx.x, lane = tid & 63;
  const int wave = __builtin_amdgcn_readfirstlane(tid >> 6);
  const int bid = blockIdx.x;
  const int xcd = bid & 7, idx = bid >> 3;
  const int m256 = (xcd >> 2) * 4 + (idx >> 3), sl = (xcd & 3) * 8 + (idx & 7);
  const int mt = m256 * 8 + wave;
  const unsigned voff = lane * 16;
  const unsigned lds0 = (unsigned)(uintptr_t)(LDSP char*)sm;
  const char* wsrc = ws + OFF_W1 + (size_t)sl * 324 * 1024 + wave * 4608;
  const unsigned wdst = lds0 + wave * 4608;
  const char* xsrc = ws + OFF_XF + (size_t)mt * 36864;
  u32x4 X[36];
  {
    unsigned keep;
    const char* csrc = ws + OFF_CC + (size_t)sl * 6912 + wave * 864;
    const unsigned cdst = lds0 + K3_CCRAW + wave * 864;
    asm volatile(
        "s_mov_b32 %0, m0\n\t"
        "s_mov_b32 m0, %3\n\t"
        "s_mov_b32 exec_hi, 0x3fffff\n\t"
        "global_load_lds_dwordx4 %1, %2\n\t"
        "s_mov_b32 exec_hi, -1\n\t"
        "s_mov_b32 m0, %0"
        : "=&s"(keep)
        : "v"(voff), "s"(csrc), "s"(cdst)
        : "memory");
  }
#define LDX4(i) { const char* b_ = xsrc + (i) * 1024; ld_frag<0>(X[i], b_, voff); ld_frag<1024>(X[(i) + 1], b_, voff); \
                  ld_frag<2048>(X[(i) + 2], b_, voff); ld_frag<3072>(X[(i) + 3], b_, voff); }
  dma_unit(wsrc, voff, wdst);
  LDX4(0) LDX4(4) LDX4(8)
  dma_unit(wsrc + UNIT, voff, wdst + UNIT);
  LDX4(12) LDX4(16) LDX4(20)
  dma_unit(wsrc + 2 * UNIT, voff, wdst + 2 * UNIT);
  LDX4(24) LDX4(28) LDX4(32)
  dma_unit(wsrc + 3 * UNIT, voff, wdst + 3 * UNIT);

  f32x16 acc[3];
#pragma unroll
  for (int ti = 0; ti < 3; ++ti)
#pragma unroll
    for (int r = 0; r < 16; ++r) acc[ti][r] = 0.f;
  char* hdst = ws + OFF_H2 + ((size_t)(mt * 2 + ((lane >> 4) & 1)) * 144 * 64 + (lane >> 5) * 16 + (lane & 15)) * 16;
  const int ft0 = sl * 9;
  float s1[4] = {0.f, 0.f, 0.f, 0.f}, s2[4] = {0.f, 0.f, 0.f, 0.f};
  const bf16x2 ones = __builtin_bit_cast(bf16x2, 0x3f803f80u);
#define STATS(b) _Pragma("unroll") for (int i_ = (b); i_ < (b) + 12; ++i_) _Pragma("unroll") for (int e_ = 0; e_ < 4; ++e_) { \
    const unsigned xu_ = X[i_][e_]; const bf16x2 xx_ = __builtin_bit_cast(bf16x2, xu_);                            \
    s1[e_] = __builtin_amdgcn_fdot2_f32_bf16(xx_, ones, s1[e_], false);                                            \
    s2[e_] = __builtin_amdgcn_fdot2_f32_bf16(xx_, xx_, s2[e_], false); }
#define PINX(b) { PIN4(X[b], X[(b) + 1], X[(b) + 2], X[(b) + 3]); PIN4(X[(b) + 4], X[(b) + 5], X[(b) + 6], X[(b) + 7]); \
                  PIN4(X[(b) + 8], X[(b) + 9], X[(b) + 10], X[(b) + 11]); }
#define BARRIER() { asm volatile("s_waitcnt lgkmcnt(0)" ::: "memory"); __builtin_amdgcn_s_barrier(); asm volatile("" ::: "memory"); }

  WAIT_VM(39); PINX(0); BARRIER();
  if (tid < 144) {
    const f32x4 a = *(const f32x4*)(sm + K3_CCRAW + tid * 16), b = *(const f32x4*)(sm + K3_CCRAW + 2304 + tid * 16),
                c = *(const f32x4*)(sm + K3_CCRAW + 4608 + tid * 16);
    *(f32x4*)(sm + K3_CC + tid * 16) = a + b + c;
  }
  STATS(0);
  k3_unit<0>(sm, lane, X, acc);
  WAIT_VM(22); PINX(12); BARRIER();
  dma_unit(wsrc + 4 * UNIT, voff, wdst);
  STATS(12);
  k3_unit<1>(sm, lane, X, acc);
  WAIT_VM(10); PINX(24); BARRIER();
  dma_unit(wsrc + 5 * UNIT, voff, wdst + UNIT);
  STATS(24);
  k3_unit<2>(sm, lane, X, acc);
  float m2, rsd;
  {
    float a = (s1[0] + s1[1]) + (s1[2] + s1[3]), b = (s2[0] + s2[1]) + (s2[2] + s2[3]);
    a += __shfl_xor(a, 32); b += __shfl_xor(b, 32);
    const float mean = a * (1.f / 576.f);
    const float var = fmaxf(b * (1.f / 576.f) - mean * mean, 0.f);
    m2 = bfr(mean);
    rsd = 1.f / bfr(sqrtf(bfr(bfr(var) + 1e-5f)));
  }
  k3_epi(sm, lane, 0, m2, rsd, acc, hdst, ft0);
  WAIT_VM(13); BARRIER();
  dma_unit(wsrc + 6 * UNIT, voff, wdst + 2 * UNIT);
  k3_unit<3>(sm, lane, X, acc);
  WAIT_VM(13); BARRIER();
  dma_unit(wsrc + 7 * UNIT, voff, wdst + 3 * UNIT);
  k3_unit<4>(sm, lane, X, acc);
  WAIT_VM(13); BARRIER();
  dma_unit(wsrc + 8 * UNIT, voff, wdst);
  k3_unit<5>(sm, lane, X, acc);
  k3_epi(sm, lane, 1, m2, rsd, acc, hdst, ft0);
  WAIT_VM(13); BARRIER();
  k3_unit<6>(sm, lane, X, acc);
  WAIT_VM(8); BARRIER();
  k3_unit<7>(sm, lane, X, acc);
  WAIT_VM(3); BARRIER();
  k3_unit<8>(sm, lane, X, acc);
  k3_epi(sm, lane, 2, m2, rsd, acc, hdst, ft0);
}

constexpr int K4_LDS = 4 * UNIT;

template <int U>
__device__ __forceinline__ void k4_unit(const char* sm, int lane, int wk, const u32x4 (&H)[36], f32x4 (&acc)[2][9]) {
  const char* slot = sm + (U & 3) * UNIT + wk * 9216 + lane * 16;
#pragma unroll
  for (int t = 0; t < 2; ++t) {
#pragma unroll
    for (int dt = 0; dt < 9; ++dt) {
      const bf16x8 a = *(const bf16x8*)(slot + t * 18432 + dt * 1024);
      acc[0][dt] = __builtin_amdgcn_mfma_f32_16x16x32_bf16(a, __builtin_bit_cast(bf16x8, H[U * 4 + t * 2]), acc[0][dt], 0, 0, 0);
      acc[1][dt] = __builtin_amdgcn_mfma_f32_16x16x32_bf16(a, __builtin_bit_cast(bf16x8, H[U * 4 + t * 2 + 1]), acc[1][dt], 0, 0, 0);
    }
  }
}

__global__ __launch_bounds__(512, 2) void k4_gemm2(char* __restrict__ ws, u16* __restrict__ out) {
  extern __shared__ __attribute__((aligned(16))) char sm[];
  const int tid = threadIdx.x, lane = tid & 63;
  const int wave = __builtin_amdgcn_readfirstlane(tid >> 6);
  const int wm = wave & 3, wk = wave >> 2;
  const int bid = blockIdx.x;
  const int xcd = bid & 7, idx = bid >> 3;
  const int kb = xcd & 3;
  const int m128 = (xcd >> 2) * 8 + (idx >> 2), nt = idx & 3;
  const unsigned voff = lane * 16;
  const unsigned lds0 = (unsigned)(uintptr_t)(LDSP char*)sm;
  const char* wsrc = ws + OFF_W2 + (size_t)(kb * 4 + nt) * 324 * 1024 + wave * 4608;
  const unsigned wdst = lds0 + wave * 4608;
  const char* h0 = ws + OFF_H2 + ((size_t)(m128 * 8 + wm * 2) * 144 + kb * 36 + wk) * 1024;
  const char* h1 = h0 + 144 * 1024;
  u32x4 H[36];
#define LDH(u) { ld_frag<0>(H[(u) * 4], h0 + (u) * 4096, voff); ld_frag<0>(H[(u) * 4 + 1], h1 + (u) * 4096, voff); \
                 ld_frag<2048>(H[(u) * 4 + 2], h0 + (u) * 4096, voff); ld_frag<2048>(H[(u) * 4 + 3], h1 + (u) * 4096, voff); }
  dma_unit(wsrc, voff, wdst);                       LDH(0)
  dma_unit(wsrc + UNIT, voff, wdst + UNIT);         LDH(1)
  dma_unit(wsrc + 2 * UNIT, voff, wdst + 2 * UNIT); LDH(2)
  dma_unit(wsrc + 3 * UNIT, voff, wdst + 3 * UNIT); LDH(3)
  f32x4 acc[2][9];
#pragma unroll
  for (int i = 0; i < 2; ++i)
#pragma unroll
    for (int dt = 0; dt < 9; ++dt) acc[i][dt] = (f32x4){0.f, 0.f, 0.f, 0.f};
#define PINH(u) PIN4(H[(u) * 4], H[(u) * 4 + 1], H[(u) * 4 + 2], H[(u) * 4 + 3])
  WAIT_VM(27); PINH(0); BARRIER();
  k4_unit<0>(sm, lane, wk, H, acc);
  WAIT_VM(18); PINH(1); BARRIER();
  dma_unit(wsrc + 4 * UNIT, voff, wdst); LDH(4)
  k4_unit<1>(sm, lane, wk, H, acc);
  WAIT_VM(18); PINH(2); BARRIER();
  dma_unit(wsrc + 5 * UNIT, voff, wdst + UNIT); LDH(5)
  k4_unit<2>(sm, lane, wk, H, acc);
  WAIT_VM(18); PINH(3); BARRIER();
  dma_unit(wsrc + 6 * UNIT, voff, wdst + 2 * UNIT); LDH(6)
  k4_unit<3>(sm, lane, wk, H, acc);
  WAIT_VM(18); PINH(4); BARRIER();
  dma_unit(wsrc + 7 * UNIT, voff, wdst + 3 * UNIT); LDH(7)
  k4_unit<4>(sm, lane, wk, H, acc);
  WAIT_VM(18); PINH(5); BARRIER();
  dma_unit(wsrc + 8 * UNIT, voff, wdst); LDH(8)
  k4_unit<5>(sm, lane, wk, H, acc);
  WAIT_VM(18); PINH(6); BARRIER();
  k4_unit<6>(sm, lane, wk, H, acc);
  WAIT_VM(9); PINH(7); BARRIER();
  k4_unit<7>(sm, lane, wk, H, acc);
  WAIT_VM(0); PINH(8); BARRIER();
  k4_unit<8>(sm, lane, wk, H, acc);
  f32x4 mine[9];
  {
    char* xb = sm + UNIT;
    char* wp = xb + wave * 9216 + lane * 16;
    if (wk == 0) {
#pragma unroll
      for (int dt = 0; dt < 9; ++dt) { *(f32x4*)(wp + dt * 1024) = acc[1][dt]; mine[dt] = acc[0][dt]; }
    } else {
#pragma unroll
      for (int dt = 0; dt < 9; ++dt) { *(f32x4*)(wp + dt * 1024) = acc[0][dt]; mine[dt] = acc[1][dt]; }
    }
    BARRIER();
    const char* rp = xb + (wave ^ 4) * 9216 + lane * 16;
#pragma unroll
    for (int dt = 0; dt < 9; ++dt) mine[dt] += *(const f32x4*)(rp + dt * 1024);
  }
  const int tile = m128 * 4 + nt;
  __amdgpu_buffer_rsrc_t prs = __builtin_amdgcn_make_buffer_rsrc((void*)(ws + OFF_PART + (size_t)tile * 4 * 73728), 0, 4 * 73728, 0x00020000);
  if (wm != kb) {
    const unsigned o = (unsigned)(kb * 73728 + (wm * 2 + wk) * 9216 + lane * 16);
#pragma unroll
    for (int dt = 0; dt < 9; ++dt)
      __builtin_amdgcn_raw_buffer_store_b128(__builtin_bit_cast(u32x4, mine[dt]), prs, o + dt * 1024, 0, 16);
  }
  WAIT_VM(0);
  BARRIER();
  GLBP unsigned* cnt = (GLBP unsigned*)(ws + OFF_CNT + tile * 128);
  if (tid == 0) __hip_atomic_fetch_add(cnt, 1u, __ATOMIC_RELAXED, __HIP_MEMORY_SCOPE_AGENT);
  if (wm != kb) return;
  const int m = m128 * 128 + wm * 32 + wk * 16 + (lane & 15);
  const int g = lane >> 4;
  u32x2 xr[9];
  f32x4 gbv[9];
#pragma unroll
  for (int dt = 0; dt < 9; ++dt) {
    const int dd = (nt * 9 + dt) * 16 + g * 4;
    xr[dt] = *(const u32x2*)(ws + OFF_XF + ((size_t)((m >> 5) * 36 + (dd >> 4)) * 64 + ((dd >> 3) & 1) * 32 + (m & 31)) * 16 + ((dd >> 2) & 1) * 8);
    gbv[dt] = *(const f32x4*)(ws + OFF_MOD + (5 * 576 + dd) * 4);
  }
  for (unsigned spins = 0; spins < (1u << 22); ++spins) {
    if ((unsigned)__builtin_amdgcn_readfirstlane(__hip_atomic_load(cnt, __ATOMIC_RELAXED, __HIP_MEMORY_SCOPE_AGENT)) >= 4u) break;
    __builtin_amdgcn_s_sleep(1);
  }
  __builtin_amdgcn_fence(__ATOMIC_ACQUIRE, "wavefront");
  u32x4 pp[3][9];
#pragma unroll
  for (int j = 0; j < 3; ++j) {
    const int src = (kb + 1 + j) & 3;
    const unsigned o = (unsigned)(src * 73728 + (wm * 2 + wk) * 9216 + lane * 16);
#pragma unroll
    for (int dt = 0; dt < 9; ++dt) pp[j][dt] = __builtin_amdgcn_raw_buffer_load_b128(prs, o + dt * 1024, 0, 16);
  }
#pragma unroll
  for (int dt = 0; dt < 9; ++dt) {
    const f32x4 r = (mine[dt] + __builtin_bit_cast(f32x4, pp[0][dt])) +
                    (__builtin_bit_cast(f32x4, pp[1][dt]) + __builtin_bit_cast(f32x4, pp[2][dt]));
    u32x2 o;
#pragma unroll
    for (int e = 0; e < 2; ++e) {
      float a = r[2 * e], b = r[2 * e + 1]; bfr2(a, b);
      a *= gbv[dt][2 * e]; b *= gbv[dt][2 * e + 1]; bfr2(a, b);
      o[e] = pk2(lo16(xr[dt][e]) + a, hi16(xr[dt][e]) + b);
    }
    const int dd = (nt * 9 + dt) * 16 + g * 4;
    *(u32x2*)(out + (size_t)m * D_ + dd) = o;
  }
}

extern "C" void kernel_launch(void* const* d_in, const int* in_sizes, int n_in, void* d_out, int out_size,
                              void* d_ws, size_t ws_size, hipStream_t stream) {
  const u16* A_q = (const u16*)d_in[0];
  const u16* A_k = (const u16*)d_in[1];
  const u16* A_v = (const u16*)d_in[2];
  const u16* A_sh = (const u16*)d_in[3];
  const u16* A_sk = (const u16*)d_in[4];
  const u16* A_D = (const u16*)d_in[5];
  const u16* Wg = (const u16*)d_in[7];
  const u16* Wu = (const u16*)d_in[8];
  const u16* Wd = (const u16*)d_in[9];
  const u16* cv = (const u16*)d_in[10];
  const u16* wm = (const u16*)d_in[11];
  char* ws = (char*)d_ws;
  (void)hipFuncSetAttribute((const void*)k1_conv, hipFuncAttributeMaxDynamicSharedMemorySize, K1_LDS);
  (void)hipFuncSetAttribute((const void*)k3_gemm1, hipFuncAttributeMaxDynamicSharedMemorySize, K3_LDS);
  (void)hipFuncSetAttribute((const void*)k4_gemm2, hipFuncAttributeMaxDynamicSharedMemorySize, K4_LDS);
  k0_prep<<<K0_GRID, 256, 0, stream>>>(A_q, A_k, A_v, A_sh, cv, wm, Wd, ws);
  k1_conv<<<K1_CONV + K1_W, 512, K1_LDS, stream>>>(A_sk, A_D, Wg, Wu, ws);
  k3_gemm1<<<256, 512, K3_LDS, stream>>>(ws);
  k4_gemm2<<<256, 512, K4_LDS, stream>>>(ws, (u16*)d_out);
}
```

```cpp
#include <hip/hip_runtime.h>
#include <hip/hip_bf16.h>
#include <stdint.h>

typedef unsigned short u16;
typedef __attribute__((ext_vector_type(2))) __bf16 bf16x2;
typedef __attribute__((ext_vector_type(8))) __bf16 bf16x8;
typedef __attribute__((ext_vector_type(2))) float f32x2;
typedef __attribute__((ext_vector_type(4))) float f32x4;
typedef __attribute__((ext_vector_type(16))) float f32x16;
typedef __attribute__((ext_vector_type(4))) unsigned u32x4;
typedef __attribute__((ext_vector_type(2))) unsigned u32x2;
typedef __attribute__((ext_vector_type(4))) short s16x4;
#define LDSP __attribute__((address_space(3)))
#define GLBP __attribute__((address_space(1)))

constexpr int S_ = 2048, D_ = 576, F_ = 4608;

constexpr size_t OFF_CNT = 0;
constexpr size_t OFF_MOD = 8192;
constexpr size_t OFF_ST1 = OFF_MOD + 16384;
constexpr size_t OFF_ST2 = OFF_ST1 + 8192;
constexpr size_t OFF_CC  = OFF_ST2 + 8192;
constexpr size_t OFF_UT  = OFF_CC + 32 * 6912;
constexpr size_t OFF_XT  = OFF_UT + (size_t)D_ * S_ * 2;
constexpr size_t OFF_XF  = OFF_XT + (size_t)D_ * S_ * 2;
constexpr size_t OFF_XS  = OFF_XF + (size_t)D_ * S_ * 2;
constexpr size_t OFF_W2  = OFF_XS + (size_t)D_ * S_ * 2;
constexpr size_t OFF_H2  = OFF_W2 + (size_t)D_ * F_ * 2;
constexpr size_t OFF_PART = OFF_H2 + (size_t)S_ * F_ * 2;

__device__ __forceinline__ unsigned pk2(float a, float b) {
  f32x2 v = {a, b};
  return __builtin_bit_cast(unsigned, __builtin_convertvector(v, bf16x2));
}
__device__ __forceinline__ float lo16(unsigned u) { return __uint_as_float(u << 16); }
__device__ __forceinline__ float hi16(unsigned u) { return __uint_as_float(u & 0xffff0000u); }
__device__ __forceinline__ float bfr(float a) { return lo16(pk2(a, a)); }
__device__ __forceinline__ void bfr2(float& a, float& b) { unsigned u = pk2(a, b); a = lo16(u); b = hi16(u); }
__device__ __forceinline__ float bf2f(u16 x) { return __uint_as_float(((unsigned)x) << 16); }
__device__ __forceinline__ s16x4 tr_read(const char* p) {
  return __builtin_amdgcn_ds_read_tr16_b64_v4i16((LDSP s16x4*)p);
}
__device__ __forceinline__ u32x4 tr_read2(const char* p0, const char* p1) {
  s16x4 a = tr_read(p0), b = tr_read(p1);
  u32x2 x = __builtin_bit_cast(u32x2, a), y = __builtin_bit_cast(u32x2, b);
  u32x4 r = {x[0], x[1], y[0], y[1]};
  return r;
}

typedef LDSP const char* lptr;
__device__ __forceinline__ u32x4 tr_read2l(lptr p0, lptr p1) {
  s16x4 a = __builtin_amdgcn_ds_read_tr16_b64_v4i16((LDSP s16x4*)p0), b = __builtin_amdgcn_ds_read_tr16_b64_v4i16((LDSP s16x4*)p1);
  u32x2 x = __builtin_bit_cast(u32x2, a), y = __builtin_bit_cast(u32x2, b);
  u32x4 r = {x[0], x[1], y[0], y[1]};
  return r;
}

constexpr int K0_MOD = 108, K0_KV = 288, K0_ST = 64, K0_WD = 144;
constexpr int K0_GRID = K0_MOD + K0_KV + K0_ST + K0_WD;

__global__ __launch_bounds__(256) void k0_prep(const u16* __restrict__ xq, const u16* __restrict__ kk,
                                               const u16* __restrict__ vv, const u16* __restrict__ shift_k,
                                               const u16* __restrict__ cvec, const u16* __restrict__ w_mod,
                                               const u16* __restrict__ Wd, char* __restrict__ ws) {
  __shared__ __attribute__((aligned(16))) char sm[37888];
  const int tid = threadIdx.x, lane = tid & 63, wave = tid >> 6;
  int bid = blockIdx.x;
  if (bid < K0_MOD) {
    if (bid == 0 && tid < 64) *(unsigned*)(ws + OFF_CNT + tid * 128) = 0u;
    if (bid >= 1 && bid <= 8) ((unsigned*)(ws + OFF_ST2))[(bid - 1) * 256 + tid] = 0u;
    const int c4 = tid & 3, rg = tid >> 2;
    u32x4 w[9];
    float sc[9];
#pragma unroll
    for (int i = 0; i < 9; ++i) {
      const int row = rg + 64 * i;
      w[i] = *(const u32x4*)(w_mod + (size_t)row * 3456 + bid * 32 + c4 * 8);
      sc[i] = bf2f(cvec[row]);
    }
    float acc[8];
#pragma unroll
    for (int e = 0; e < 8; ++e) acc[e] = 0.f;
#pragma unroll
    for (int i = 0; i < 9; ++i) {
      const float s = bfr(sc[i] / (1.f + __expf(-sc[i])));
#pragma unroll
      for (int e = 0; e < 4; ++e) {
        acc[2 * e] += s * lo16(w[i][e]);
        acc[2 * e + 1] += s * hi16(w[i][e]);
      }
    }
#pragma unroll
    for (int e = 0; e < 8; ++e) {
#pragma unroll
      for (int o = 4; o < 64; o <<= 1) acc[e] += __shfl_xor(acc[e], o);
    }
    float* red = (float*)sm;
    if (lane < 4) {
#pragma unroll
      for (int e = 0; e < 8; ++e) red[wave * 32 + lane * 8 + e] = acc[e];
    }
    __syncthreads();
    if (tid < 32) {
      const float r = (red[tid] + red[32 + tid]) + (red[64 + tid] + red[96 + tid]);
      ((float*)(ws + OFF_MOD))[bid * 32 + tid] = bfr(r);
    }
    return;
  }
  bid -= K0_MOD;
  if (bid < K0_KV) {
    const int cb = bid % 9, tb = bid / 9;
    const int c8 = tid & 7, rp = tid >> 3;
    const int r0 = tb * 64 + rp * 2;
    const int ch0 = cb * 64 + c8 * 8;
    u32x4 kr[5], vr[2], xr[2], sw[4];
#pragma unroll
    for (int i = 0; i < 5; ++i) {
      const int r = r0 - 3 + i;
      const int rc = r < 0 ? 0 : r;
      kr[i] = *(const u32x4*)(kk + (size_t)rc * D_ + ch0);
      if (r < 0) kr[i] = (u32x4){0u, 0u, 0u, 0u};
    }
#pragma unroll
    for (int i = 0; i < 2; ++i) {
      vr[i] = *(const u32x4*)(vv + (size_t)(r0 + i) * D_ + ch0);
      xr[i] = *(const u32x4*)(xq + (size_t)(r0 + i) * D_ + ch0);
    }
#pragma unroll
    for (int i = 0; i < 4; ++i) sw[i] = *(const u32x4*)(shift_k + (size_t)ch0 * 4 + i * 8);
    char* tu = sm;
    char* tx = sm + 8192;
    const int q = rp >> 2, inner = rp & 3;
#pragma unroll
    for (int e = 0; e < 8; ++e) {
      const unsigned wa = sw[e >> 1][(e & 1) * 2], wb = sw[e >> 1][(e & 1) * 2 + 1];
      const float w0 = lo16(wa), w1 = hi16(wa), w2 = lo16(wb), w3 = hi16(wb);
      float kf[5];
#pragma unroll
      for (int i = 0; i < 5; ++i) kf[i] = (e & 1) ? hi16(kr[i][e >> 1]) : lo16(kr[i][e >> 1]);
      const float v0 = (e & 1) ? hi16(vr[0][e >> 1]) : lo16(vr[0][e >> 1]);
      const float v1 = (e & 1) ? hi16(vr[1][e >> 1]) : lo16(vr[1][e >> 1]);
      float a0 = kf[0] * w3, a1 = kf[1] * w3; bfr2(a0, a1);
      float t0 = kf[1] * w2, t1 = kf[2] * w2; bfr2(t0, t1);
      a0 += t0; a1 += t1; bfr2(a0, a1);
      t0 = kf[2] * w1; t1 = kf[3] * w1; bfr2(t0, t1);
      a0 += t0; a1 += t1; bfr2(a0, a1);
      t0 = kf[3] * w0; t1 = kf[4] * w0; bfr2(t0, t1);
      a0 += t0; a1 += t1; bfr2(a0, a1);
      const unsigned kvp = pk2(a0 * v0, a1 * v1);
      const unsigned x0 = xr[0][e >> 1], x1 = xr[1][e >> 1];
      const unsigned xp = (e & 1) ? ((x0 >> 16) | (x1 & 0xffff0000u)) : ((x0 & 0xffffu) | (x1 << 16));
      const int off = (c8 * 8 + e) * 128 + ((q ^ c8) * 16) + inner * 4;
      *(unsigned*)(tu + off) = kvp;
      *(unsigned*)(tx + off) = xp;
    }
    __syncthreads();
#pragma unroll
    for (int i = 0; i < 2; ++i) {
      const int p = tid + 256 * i, ch = p >> 3, qq = p & 7;
      const int off = ch * 128 + ((qq ^ (ch >> 3)) * 16);
      const size_t g = ((size_t)(cb * 64 + ch) * S_ + tb * 64 + qq * 8) * 2;
      *(u32x4*)(ws + OFF_UT + g) = *(const u32x4*)(tu + off);
      *(u32x4*)(ws + OFF_XT + g) = *(const u32x4*)(tx + off);
    }
    return;
  }
  bid -= K0_KV;
  if (bid < K0_ST) {
    const int row = bid * 32 + wave * 8 + (lane >> 3), c = lane & 7;
    u32x4 xr[9];
#pragma unroll
    for (int i = 0; i < 9; ++i) xr[i] = *(const u32x4*)(xq + (size_t)row * D_ + (c + 8 * i) * 8);
    float s = 0.f;
#pragma unroll
    for (int i = 0; i < 9; ++i)
#pragma unroll
      for (int e = 0; e < 4; ++e) s += lo16(xr[i][e]) + hi16(xr[i][e]);
    s += __shfl_xor(s, 1); s += __shfl_xor(s, 2); s += __shfl_xor(s, 4);
    const float mean = s * (1.f / 576.f);
    float ss = 0.f;
#pragma unroll
    for (int i = 0; i < 9; ++i)
#pragma unroll
      for (int e = 0; e < 4; ++e) {
        const float a = lo16(xr[i][e]) - mean, b = hi16(xr[i][e]) - mean;
        ss += a * a + b * b;
      }
    ss += __shfl_xor(ss, 1); ss += __shfl_xor(ss, 2); ss += __shfl_xor(ss, 4);
    const float var = bfr(ss * (1.f / 576.f));
    const float sd = sqrtf(bfr(var + 1e-5f));
    if (c == 0) ((unsigned*)(ws + OFF_ST1))[row] = pk2(mean, sd);
    return;
  }
  bid -= K0_ST;
  {
    const int T = bid;
    const char* src = (const char*)Wd + (size_t)T * 32 * 1152;
    u32x4 w[9];
#pragma unroll
    for (int i = 0; i < 9; ++i) w[i] = *(const u32x4*)(src + (tid + 256 * i) * 16);
#pragma unroll
    for (int i = 0; i < 9; ++i) {
      const int p = tid + 256 * i, row = p / 72, cc = p % 72;
      *(u32x4*)(sm + row * 1184 + cc * 16) = w[i];
    }
    __syncthreads();
    const int g = lane >> 4, q = (lane & 15) >> 2, p = lane & 3;
    const char* base = sm + (16 * (g >> 1) + 4 * (g & 1) + q) * 1184 + (wave * 144 + 4 * p) * 2;
    char* dst = ws + OFF_W2 + ((size_t)(((T / 36) * 4 + wave) * 36 + (T % 36)) * 9) * 1024 + lane * 16;
#pragma unroll
    for (int i = 0; i < 9; ++i) {
      const u32x4 r = tr_read2(base + i * 32, base + i * 32 + 8 * 1184);
      *(u32x4*)(dst + i * 1024) = r;
    }
  }
}

constexpr int K1_CONV = 144, K1_W = 108;
constexpr int KC_STRIDE = 4416;
constexpr int CH_BYTES = 4 * KC_STRIDE + 4672;
constexpr int K1_XCH = 4 * CH_BYTES;
constexpr int K1_X1 = K1_XCH + 32768;
constexpr int K1_LDS = K1_X1 + 32768;

__global__ __launch_bounds__(512, 2) void k1_conv(const u16* __restrict__ ssm_k, const u16* __restrict__ ssm_D,
                                                  const u16* __restrict__ Wg, const u16* __restrict__ Wu,
                                                  char* __restrict__ ws) {
  extern __shared__ __attribute__((aligned(16))) char sm[];
  const int tid = threadIdx.x, lane = tid & 63, wave = tid >> 6;
  const float* mod = (const float*)(ws + OFF_MOD);
  int bid = blockIdx.x;
  if (bid < K1_CONV) {
    const int cg = (bid & 7) * 18 + (bid >> 3);
    const int cl = wave >> 1, w2 = wave & 1;
    const int d = cg * 4 + cl;
    char* chb = sm + cl * CH_BYTES;
    char* ub = chb + 4 * KC_STRIDE;
    const int t128 = tid & 127;
    const char* krow = (const char*)ssm_k + (size_t)d * 4096;
    const char* urow = ws + OFF_UT + (size_t)d * 4096;
    const char* xrow = ws + OFF_XT + (size_t)d * 4096;
    u32x4 ka[2], ua[2];
    u32x2 kb[2];
#pragma unroll
    for (int i = 0; i < 2; ++i) {
      const int c = t128 + 128 * i;
      ka[i] = *(const u32x4*)(krow + c * 16);
      const int cn = c < 255 ? c + 1 : 255;
      kb[i] = *(const u32x2*)(krow + cn * 16);
      if (c == 255) kb[i] = (u32x2){0u, 0u};
      ua[i] = *(const u32x4*)(urow + c * 16);
    }
    const int li = lane & 15, g = lane >> 4;
    u32x2 xv[4];
    u32x4 st[4];
#pragma unroll
    for (int i = 0; i < 4; ++i) {
      const int s = 256 * (4 * w2 + i) + 16 * li + 4 * g;
      xv[i] = *(const u32x2*)(xrow + s * 2);
      st[i] = *(const u32x4*)(ws + OFF_ST1 + s * 4);
    }
    const float sa = mod[d], c1a = bfr(1.f + mod[576 + d]), ga = mod[1152 + d];
    const float Dd = bf2f(ssm_D[d]);
    const float c1b = bfr(1.f + mod[4 * 576 + d]);
    {
      const u32x4 z = {0u, 0u, 0u, 0u};
      if (t128 < 32) *(u32x4*)(ub + t128 * 16) = z;
      else if (t128 < 32 + 12) {
        const int i = t128 - 32;
        *(u32x4*)(chb + (i / 3) * KC_STRIDE + (i % 3) * 16) = z;
      } else if (t128 < 32 + 12 + 16) {
        const int i = t128 - 44;
        *(u32x4*)(chb + (i >> 2) * KC_STRIDE + 64 + 4096 + (i & 3) * 16) = z;
      } else if (t128 < 64) {
        *(u32x4*)(ub + 4608 + (t128 - 60) * 16) = z;
      }
    }
#pragma unroll
    for (int i = 0; i < 2; ++i) {
      const int c = t128 + 128 * i;
      const unsigned w0 = ka[i][0], w1 = ka[i][1], w2_ = ka[i][2], w3 = ka[i][3], w4 = kb[i][0], w5 = kb[i][1];
      const unsigned a0 = __builtin_amdgcn_alignbit(w1, w0, 16), a1 = __builtin_amdgcn_alignbit(w2_, w1, 16),
                     a2 = __builtin_amdgcn_alignbit(w3, w2_, 16), a3 = __builtin_amdgcn_alignbit(w4, w3, 16),
                     a4 = __builtin_amdgcn_alignbit(w5, w4, 16);
      char* p = chb + 64 + c * 16;
      *(u32x4*)(p) = ka[i];
      *(u32x4*)(p + KC_STRIDE) = (u32x4){a0, a1, a2, a3};
      *(u32x4*)(p + 2 * KC_STRIDE) = (u32x4){w1, w2_, w3, w4};
      *(u32x4*)(p + 3 * KC_STRIDE) = (u32x4){a1, a2, a3, a4};
      *(u32x4*)(ub + 512 + c * 16) = ua[i];
      if (c == 0) {
        *(u32x4*)(p - 16) = (u32x4){0u, 0u, 0u, 0u};
        *(u32x4*)(p - 16 + KC_STRIDE) = (u32x4){0u, 0u, 0u, w0 << 16};
        *(u32x4*)(p - 16 + 2 * KC_STRIDE) = (u32x4){0u, 0u, 0u, w0};
        *(u32x4*)(p - 16 + 3 * KC_STRIDE) = (u32x4){0u, 0u, w0 << 16, a0};
      }
    }
    __syncthreads();
    const int q = li >> 2, p4 = li & 3;
    const char* abase = chb + ((4 - q) & 3) * KC_STRIDE + 2 * (32 - 8 * g - (q ? 4 : 0) + 4 * p4) - 8;
    const char* bbase = ub + 2 * (256 + 16 * li + 8 * g);
    f32x4 acc[8];
#pragma unroll
    for (int b = 0; b < 8; ++b) acc[b] = (f32x4){0.f, 0.f, 0.f, 0.f};
#pragma unroll 1
    for (int it = 0; it < 4; ++it) {
      const int r = 2 * it + 1 + w2;
      const char* ap = abase + 64 * r;
      const char* bp = bbase - 64 * r;
      bf16x8 A[8], B[8];
#pragma unroll
      for (int i = 0; i < 8; ++i) {
        A[i] = __builtin_bit_cast(bf16x8, tr_read2(ap + 512 * i + 8, ap + 512 * i));
        B[i] = *(const bf16x8*)(bp + 512 * i);
      }
#pragma unroll
      for (int b = 0; b < 8; ++b)
#pragma unroll
        for (int qq = 0; qq <= b; ++qq)
          acc[b] = __builtin_amdgcn_mfma_f32_16x16x32_bf16(A[qq], B[b - qq], acc[b], 0, 0, 0);
    }
    char* xch = sm + K1_XCH;
    f32x4 fin[4];
    if (w2 == 0) {
#pragma unroll
      for (int i = 0; i < 4; ++i) { *(f32x4*)(xch + wave * 4096 + i * 1024 + lane * 16) = acc[4 + i]; fin[i] = acc[i]; }
    } else {
#pragma unroll
      for (int i = 0; i < 4; ++i) { *(f32x4*)(xch + wave * 4096 + i * 1024 + lane * 16) = acc[i]; fin[i] = acc[4 + i]; }
    }
    {
      const bf16x8 A0 = __builtin_bit_cast(bf16x8, tr_read2(abase + 8, abase));
#pragma unroll
      for (int i = 0; i < 4; ++i) {
        const bf16x8 Bd = *(const bf16x8*)(bbase + 512 * (4 * w2 + i));
        fin[i] = __builtin_amdgcn_mfma_f32_16x16x32_bf16(A0, Bd, fin[i], 0, 0, 0);
      }
    }
    __syncthreads();
    char* x1s = sm + K1_X1;
#pragma unroll
    for (int i = 0; i < 4; ++i) {
      const f32x4 o = *(const f32x4*)(xch + (wave ^ 1) * 4096 + i * 1024 + lane * 16);
      const int s = 256 * (4 * w2 + i) + 16 * li + 4 * g;
      const u32x2 uu = *(const u32x2*)(ub + 512 + s * 2);
#pragma unroll
      for (int e = 0; e < 2; ++e) {
        const float x0 = lo16(xv[i][e]), x1 = hi16(xv[i][e]);
        const unsigned s0 = st[i][2 * e], s1 = st[i][2 * e + 1];
        float h0 = x0 - lo16(s0), h1 = x1 - lo16(s1); bfr2(h0, h1);
        h0 = h0 * __builtin_amdgcn_rcpf(hi16(s0)); h1 = h1 * __builtin_amdgcn_rcpf(hi16(s1)); bfr2(h0, h1);
        h0 *= c1a; h1 *= c1a; bfr2(h0, h1);
        h0 += sa; h1 += sa; bfr2(h0, h1);
        float y0 = fin[i][2 * e] + o[2 * e], y1 = fin[i][2 * e + 1] + o[2 * e + 1]; bfr2(y0, y1);
        float t0 = lo16(uu[e]) * Dd, t1 = hi16(uu[e]) * Dd; bfr2(t0, t1);
        y0 += t0; y1 += t1; bfr2(y0, y1);
        y0 *= h0; y1 *= h1; bfr2(y0, y1);
        y0 *= ga; y1 *= ga; bfr2(y0, y1);
        const unsigned r = pk2(x0 + y0, x1 + y1);
        *(u16*)(x1s + (s + 2 * e) * 8 + cl * 2) = (u16)(r & 0xffffu);
        *(u16*)(x1s + (s + 2 * e + 1) * 8 + cl * 2) = (u16)(r >> 16);
        const unsigned r2 = pk2(lo16(r) * c1b, hi16(r) * c1b);
        *(u16*)(x1s + 16384 + (s + 2 * e) * 8 + cl * 2) = (u16)(r2 & 0xffffu);
        *(u16*)(x1s + 16384 + (s + 2 * e + 1) * 8 + cl * 2) = (u16)(r2 >> 16);
      }
    }
    __syncthreads();
    const int d0 = cg * 4;
    char* xf = ws + OFF_XF + ((size_t)(d0 >> 4) * 64 + ((d0 >> 3) & 1) * 32) * 16 + ((d0 >> 2) & 1) * 8;
#pragma unroll
    for (int i = 0; i < 4; ++i) {
      const int s = tid + 512 * i;
      *(u32x2*)(xf + (size_t)(s >> 5) * 36864 + (s & 31) * 16) = *(const u32x2*)(x1s + s * 8);
      *(u32x2*)(xf + (OFF_XS - OFF_XF) + (size_t)(s >> 5) * 36864 + (s & 31) * 16) = *(const u32x2*)(x1s + 16384 + s * 8);
    }
    return;
  }
  bid -= K1_CONV;
  {
    const int cbk = bid % 36, kt = bid / 36;
    const int mat = cbk / 18, cblk = cbk % 18;
    const u16* W = mat ? Wu : Wg;
    const int p = tid & 31, rg = tid >> 5;
    u32x4 w[12];
    float cs[12], sb[12];
#pragma unroll
    for (int i = 0; i < 12; ++i) {
      const int k = kt * 192 + rg + 16 * i;
      w[i] = *(const u32x4*)(W + (size_t)k * F_ + cblk * 256 + p * 8);
      cs[i] = mod[4 * 576 + k];
      sb[i] = mod[3 * 576 + k];
    }
    float a1[8], a2[8];
#pragma unroll
    for (int e = 0; e < 8; ++e) { a1[e] = 0.f; a2[e] = 0.f; }
#pragma unroll
    for (int i = 0; i < 12; ++i) {
      const float c1 = bfr(1.f + cs[i]);
#pragma unroll
      for (int e = 0; e < 4; ++e) {
        const float l = lo16(w[i][e]), h = hi16(w[i][e]);
        a1[2 * e] += c1 * l; a1[2 * e + 1] += c1 * h;
        a2[2 * e] += sb[i] * l; a2[2 * e + 1] += sb[i] * h;
      }
    }
    float* red = (float*)(sm + 110592);
#pragma unroll
    for (int e = 0; e < 8; ++e) { a1[e] += __shfl_xor(a1[e], 32); a2[e] += __shfl_xor(a2[e], 32); }
    if (lane < 32) {
      *(f32x4*)(red + wave * 512 + p * 8) = (f32x4){a1[0], a1[1], a1[2], a1[3]};
      *(f32x4*)(red + wave * 512 + p * 8 + 4) = (f32x4){a1[4], a1[5], a1[6], a1[7]};
    } else {
      *(f32x4*)(red + wave * 512 + 256 + p * 8) = (f32x4){a2[0], a2[1], a2[2], a2[3]};
      *(f32x4*)(red + wave * 512 + 256 + p * 8 + 4) = (f32x4){a2[4], a2[5], a2[6], a2[7]};
    }
    __syncthreads();
    {
      float r = 0.f;
#pragma unroll
      for (int i = 0; i < 8; ++i) r += red[i * 512 + tid];
      const int which = tid >> 8, col = tid & 255;
      const int ft = cblk * 16 + (col >> 4);
      ((float*)(ws + OFF_CC))[(ft / 9) * 1728 + kt * 576 + which * 288 + (ft % 9) * 32 + mat * 16 + (col & 15)] = r;
    }
  }
}

constexpr int UNIT = 36864;

__device__ __forceinline__ void dma_unit(const char* gbase  , unsigned voff, unsigned lds_dst  ) {
  unsigned keep;
  asm volatile(
      "s_mov_b32 %0, m0\n\t"
      "s_mov_b32 m0, %3\n\t"
      "s_mov_b32 exec_hi, 0\n\t"
      "global_load_lds_dwordx4 %1, %2\n\t"
      "s_mov_b32 exec_hi, -1\n\t"
      "global_load_lds_dwordx4 %1, %2 offset:512\n\t"
      "global_load_lds_dwordx4 %1, %2 offset:1536\n\t"
      "global_load_lds_dwordx4 %1, %2 offset:2560\n\t"
      "global_load_lds_dwordx4 %1, %2 offset:3584\n\t"
      "s_mov_b32 m0, %0"
      : "=&s"(keep)
      : "v"(voff), "s"(gbase), "s"(lds_dst)
      : "memory");
}
template <int OFF>
__device__ __forceinline__ void ld_frag(u32x4& dst, const char* gbase  , unsigned voff) {
  asm volatile("global_load_dwordx4 %0, %1, %2 offset:%3" : "=v"(dst) : "v"(voff), "s"(gbase), "i"(OFF) : "memory");
}
#define H2_AUX 16
#define BARRIER() { asm volatile("s_waitcnt lgkmcnt(0)" ::: "memory"); __builtin_amdgcn_s_barrier(); asm volatile("" ::: "memory"); }
#define WAIT_VM(n) asm volatile("s_waitcnt vmcnt(" #n ")" ::: "memory")
#define PIN4(a, b, c, d) asm volatile("" : "+v"(a), "+v"(b), "+v"(c), "+v"(d))

constexpr int K3_SLOT = 37120;
constexpr int K3_UP = 18560;
constexpr int K3_CCRAW = 4 * K3_SLOT;
constexpr int K3_CC = K3_CCRAW + 6912;
constexpr int K3_LDS = K3_CC + 2304;
constexpr int K3_D = 6;

__device__ __forceinline__ void dma_rows(const char* gbase  , const unsigned (&vo)[5], unsigned lds_dst  ) {
  unsigned keep;
  asm volatile(
      "s_mov_b32 %0, m0\n\t"
      "s_mov_b32 m0, %7\n\t"
      "s_mov_b32 exec_hi, 0\n\t"
      "global_load_lds_dwordx4 %1, %6\n\t"
      "s_mov_b32 exec_hi, -1\n\t"
      "global_load_lds_dwordx4 %2, %6 offset:512\n\t"
      "global_load_lds_dwordx4 %3, %6 offset:1536\n\t"
      "global_load_lds_dwordx4 %4, %6 offset:2560\n\t"
      "global_load_lds_dwordx4 %5, %6 offset:3584\n\t"
      "s_mov_b32 m0, %0"
      : "=&s"(keep)
      : "v"(vo[0]), "v"(vo[1]), "v"(vo[2]), "v"(vo[3]), "v"(vo[4]), "s"(gbase), "s"(lds_dst)
      : "memory");
}
#define SB0() __builtin_amdgcn_sched_barrier(0)
#define XBAR() { SB0(); asm volatile("s_barrier" ::: "memory"); SB0(); }

template <int I>
__device__ __forceinline__ void k3_rd(lptr b0, lptr b2, u32x4 (&A)[K3_D]) {
  constexpr int u = I / 36, r = I % 36, ks = r / 3, ti = r % 3;
  lptr p = ((u & 2) ? b2 : b0) + (u & 1) * K3_SLOT + ks * 1536 + ti * 32;
  A[I % K3_D] = tr_read2l(p, p + 384);
}
template <int I0, int I1>
__device__ __forceinline__ void k3_run(lptr b0, lptr b2, u32x4 (&A)[K3_D], const u32x4 (&X)[36], f32x16 (&acc)[3]) {
  if constexpr (I0 < I1) {
    constexpr int u = I0 / 36, r = I0 % 36, ks = r / 3, ti = r % 3;
    const u32x4 a = A[I0 % K3_D];
    acc[ti] = __builtin_amdgcn_mfma_f32_32x32x16_bf16(__builtin_bit_cast(bf16x8, a), __builtin_bit_cast(bf16x8, X[(u % 3) * 12 + ks]), acc[ti], 0, 0, 0);
    if constexpr (I0 + K3_D < 324) k3_rd<I0 + K3_D>(b0, b2, A);
    SB0();
    k3_run<I0 + 1, I1>(b0, b2, A, X, acc);
  }
}

template <int G, bool ADD>
__device__ __forceinline__ void k3_epi(lptr cp, float nm, float sd, f32x16 (&acc)[3], __amdgpu_buffer_rsrc_t hrs,
                                       unsigned hoff, int ft0) {
#pragma unroll
  for (int ti = 0; ti < 3; ++ti) {
    if constexpr (ADD) {
#pragma unroll
      for (int qq = 0; qq < 4; ++qq) {
        const f32x4 c1 = *(LDSP const f32x4*)(cp + (G * 3 + ti) * 128 + qq * 32);
        const f32x4 c2 = *(LDSP const f32x4*)(cp + 1152 + (G * 3 + ti) * 128 + qq * 32);
#pragma unroll
        for (int r = 0; r < 4; ++r) acc[ti][qq * 4 + r] += nm * c1[r] + sd * c2[r];
        if (qq & 1) SB0();
      }
    }
    u32x4 o;
#pragma unroll
    for (int e = 0; e < 4; ++e)
      o[e] = pk2(fmaxf(acc[ti][2 * e], 0.f) * acc[ti][8 + 2 * e], fmaxf(acc[ti][2 * e + 1], 0.f) * acc[ti][9 + 2 * e]);
    const int ft = ft0 + G * 3 + ti;
    __builtin_amdgcn_raw_buffer_store_b128(o, hrs, hoff, (ft >> 1) * 1024 + (ft & 1) * 512, H2_AUX);
    if constexpr (G < 2) {
#pragma unroll
      for (int qq = 0; qq < 4; ++qq) {
        const f32x4 c1 = *(LDSP const f32x4*)(cp + (G * 3 + 3 + ti) * 128 + qq * 32);
        const f32x4 c2 = *(LDSP const f32x4*)(cp + 1152 + (G * 3 + 3 + ti) * 128 + qq * 32);
#pragma unroll
        for (int r = 0; r < 4; ++r) acc[ti][qq * 4 + r] = nm * c1[r] + sd * c2[r];
      }
      SB0();
    }
  }
  SB0();
}

__global__ __launch_bounds__(512, 2) void k3_gemm1(const u16* __restrict__ Wg, const u16* __restrict__ Wu, char* __restrict__ ws) {
  extern __shared__ __attribute__((aligned(16))) char sm[];
  const int tid = threadIdx.x, lane = tid & 63;
  const int wave = __builtin_amdgcn_readfirstlane(tid >> 6);
  const int bid = blockIdx.x;
  const int xcd = bid & 7, idx = bid >> 3;
  const int m256 = (xcd >> 2) * 4 + (idx >> 3), sl = (xcd & 3) * 8 + (idx & 7);
  const int mt = m256 * 8 + wave;
  const unsigned voff = lane * 16;
  const unsigned lds0 = (unsigned)(uintptr_t)(LDSP char*)sm;
  u32x4 sx[9];
  if (wave == 7) {
    const int r0 = m256 * 256 + sl * 8;
    const char* sb = ws + OFF_XF + (size_t)(r0 >> 5) * 36864 + (r0 & 31) * 16;
    const unsigned so = (lane >> 3) * 512 + (lane & 7) * 16;
#pragma unroll
    for (int i = 0; i < 9; ++i) ld_frag<0>(sx[i], sb + i * 4096, so);
  }
  const char* wsrc = (const char*)((wave & 4) ? Wu : Wg) + (size_t)(wave & 3) * 48 * 9216 + sl * 288;
  const unsigned wdst = lds0 + (wave >> 2) * K3_UP + (wave & 3) * 4608;
  unsigned vo[5];
#pragma unroll
  for (int j = 0; j < 5; ++j) {
    const unsigned off = j ? 1024 * j - 512 : 0, c = off / 16 + lane;
    vo[j] = (c / 6) * 9216 + (c % 6) * 16 - off;
  }
#define K3_DMA(u) dma_rows(wsrc + (size_t)((u) % 3) * 192 * 9216 + ((u) / 3) * 96, vo, wdst + ((u) & 3) * K3_SLOT)
  const char* xsrc = ws + OFF_XS + (size_t)mt * 36864;
  u32x4 X[36];
#define LDX4(i) { const char* b_ = xsrc + (i) * 1024; ld_frag<0>(X[i], b_, voff); ld_frag<1024>(X[(i) + 1], b_, voff); \
                  ld_frag<2048>(X[(i) + 2], b_, voff); ld_frag<3072>(X[(i) + 3], b_, voff); }
  K3_DMA(0);
  LDX4(0) LDX4(4) LDX4(8)
  {
    unsigned keep;
    const char* csrc = ws + OFF_CC + (size_t)sl * 6912 + wave * 864;
    const unsigned cdst = lds0 + K3_CCRAW + wave * 864;
    asm volatile(
        "s_mov_b32 %0, m0\n\t"
        "s_mov_b32 m0, %3\n\t"
        "s_mov_b32 exec_hi, 0x3fffff\n\t"
        "global_load_lds_dwordx4 %1, %2\n\t"
        "s_mov_b32 exec_hi, -1\n\t"
        "s_mov_b32 m0, %0"
        : "=&s"(keep)
        : "v"(voff), "s"(csrc), "s"(cdst)
        : "memory");
  }
  K3_DMA(1);
  LDX4(12) LDX4(16) LDX4(20)

  f32x16 acc[3];
#pragma unroll
  for (int ti = 0; ti < 3; ++ti)
#pragma unroll
    for (int r = 0; r < 16; ++r) acc[ti][r] = 0.f;
  __amdgpu_buffer_rsrc_t hrs = __builtin_amdgcn_make_buffer_rsrc((void*)(ws + OFF_H2 + (size_t)mt * 2 * 147456), 0, 2 * 147456, 0x00020000);
  const unsigned hoff = ((lane >> 4) & 1) * 147456 + ((lane >> 5) * 16 + (lane & 15)) * 16;
  const int ft0 = sl * 9;
  lptr b0 = (lptr)sm + ((lane >> 4) & 1) * K3_UP + ((lane >> 5) * 8 + ((lane & 15) >> 2)) * 96 + (lane & 3) * 8;
  lptr b2 = b0 + 2 * K3_SLOT;
  lptr cp = (lptr)sm + K3_CC + (lane >> 5) * 16;
  asm volatile("" : "+v"(b0), "+v"(b2), "+v"(cp));
  u32x4 A[K3_D];
#define PINX(b) { PIN4(X[b], X[(b) + 1], X[(b) + 2], X[(b) + 3]); PIN4(X[(b) + 4], X[(b) + 5], X[(b) + 6], X[(b) + 7]); \
                  PIN4(X[(b) + 8], X[(b) + 9], X[(b) + 10], X[(b) + 11]); }

  WAIT_VM(18); PINX(0); XBAR();
  k3_rd<0>(b0, b2, A); k3_rd<1>(b0, b2, A); k3_rd<2>(b0, b2, A);
  k3_rd<3>(b0, b2, A); k3_rd<4>(b0, b2, A); k3_rd<5>(b0, b2, A);
  K3_DMA(2);
  if (wave == 7) {
    PIN4(sx[0], sx[1], sx[2], sx[3]); PIN4(sx[4], sx[5], sx[6], sx[7]); asm volatile("" : "+v"(sx[8]));
    const bf16x2 ones = __builtin_bit_cast(bf16x2, 0x3f803f80u);
    float s1[2] = {0.f, 0.f}, s2[2] = {0.f, 0.f};
#pragma unroll
    for (int i = 0; i < 9; ++i)
#pragma unroll
      for (int e = 0; e < 4; ++e) {
        const unsigned xu = sx[i][e];
        const bf16x2 xx = __builtin_bit_cast(bf16x2, xu);
        s1[e & 1] = __builtin_amdgcn_fdot2_f32_bf16(xx, ones, s1[e & 1], false);
        s2[e & 1] = __builtin_amdgcn_fdot2_f32_bf16(xx, xx, s2[e & 1], false);
      }
    float a = s1[0] + s1[1], b = s2[0] + s2[1];
#pragma unroll
    for (int o = 8; o < 64; o <<= 1) { a += __shfl_xor(a, o); b += __shfl_xor(b, o); }
    const float mean = a * (1.f / 576.f);
    const float var = fmaxf(b * (1.f / 576.f) - mean * mean, 0.f);
    const unsigned w = pk2(mean, sqrtf(bfr(bfr(var) + 1e-5f)));
    if (lane < 8)
      __hip_atomic_store((GLBP unsigned*)(ws + OFF_ST2) + m256 * 256 + sl * 8 + lane, w, __ATOMIC_RELAXED, __HIP_MEMORY_SCOPE_AGENT);
  }
  LDX4(24) LDX4(28) LDX4(32)
  SB0();
  k3_run<0, 30>(b0, b2, A, X, acc);
  WAIT_VM(17); PINX(12); XBAR();
  K3_DMA(3);
  if (tid < 144) {
    const f32x4 a = *(const f32x4*)(sm + K3_CCRAW + tid * 16), b = *(const f32x4*)(sm + K3_CCRAW + 2304 + tid * 16),
                c = *(const f32x4*)(sm + K3_CCRAW + 4608 + tid * 16);
    *(f32x4*)(sm + K3_CC + tid * 16) = a + b + c;
  }
  SB0();
  k3_run<30, 66>(b0, b2, A, X, acc);
  WAIT_VM(5); PINX(24); XBAR();
  unsigned stw;
  const char* stp = ws + OFF_ST2 + (size_t)mt * 128;
  const unsigned sto = (lane & 31) * 4;
  asm volatile("global_load_dword %0, %1, %2 sc1" : "=v"(stw) : "v"(sto), "s"(stp) : "memory");
  K3_DMA(4);
  k3_run<66, 102>(b0, b2, A, X, acc);
  WAIT_VM(5); asm volatile("" : "+v"(stw)); XBAR();
  K3_DMA(5);
  k3_run<102, 108>(b0, b2, A, X, acc);
  for (unsigned spins = 0; __builtin_amdgcn_ballot_w64(stw == 0u) != 0ull && spins < (1u << 20); ++spins) {
    __builtin_amdgcn_s_sleep(2);
    asm volatile("global_load_dword %0, %1, %2 sc1\n\ts_waitcnt vmcnt(0)" : "=v"(stw) : "v"(sto), "s"(stp) : "memory");
  }
  const float nm = -lo16(stw), sd = hi16(stw);
  k3_epi<0, true>(cp, nm, sd, acc, hrs, hoff, ft0);
  k3_run<108, 138>(b0, b2, A, X, acc);
  WAIT_VM(8); XBAR();
  K3_DMA(6);
  k3_run<138, 174>(b0, b2, A, X, acc);
  WAIT_VM(8); XBAR();
  K3_DMA(7);
  k3_run<174, 210>(b0, b2, A, X, acc);
  WAIT_VM(5); XBAR();
  K3_DMA(8);
  k3_run<210, 216>(b0, b2, A, X, acc);
  k3_epi<1, false>(cp, nm, sd, acc, hrs, hoff, ft0);
  k3_run<216, 246>(b0, b2, A, X, acc);
  WAIT_VM(8); XBAR();
  k3_run<246, 282>(b0, b2, A, X, acc);
  WAIT_VM(3); XBAR();
  k3_run<282, 324>(b0, b2, A, X, acc);
  k3_epi<2, false>(cp, nm, sd, acc, hrs, hoff, ft0);
}

constexpr int K4_LDS = 4 * UNIT;

template <int U>
__device__ __forceinline__ void k4_unit(const char* sm, int lane, int wk, const u32x4 (&H)[36], f32x4 (&acc)[2][9]) {
  const char* slot = sm + (U & 3) * UNIT + wk * 9216 + lane * 16;
#pragma unroll
  for (int t = 0; t < 2; ++t) {
#pragma unroll
    for (int dt = 0; dt < 9; ++dt) {
      const bf16x8 a = *(const bf16x8*)(slot + t * 18432 + dt * 1024);
      acc[0][dt] = __builtin_amdgcn_mfma_f32_16x16x32_bf16(a, __builtin_bit_cast(bf16x8, H[U * 4 + t * 2]), acc[0][dt], 0, 0, 0);
      acc[1][dt] = __builtin_amdgcn_mfma_f32_16x16x32_bf16(a, __builtin_bit_cast(bf16x8, H[U * 4 + t * 2 + 1]), acc[1][dt], 0, 0, 0);
    }
  }
}

__global__ __launch_bounds__(512, 2) void k4_gemm2(char* __restrict__ ws, u16* __restrict__ out) {
  extern __shared__ __attribute__((aligned(16))) char sm[];
  const int tid = threadIdx.x, lane = tid & 63;
  const int wave = __builtin_amdgcn_readfirstlane(tid >> 6);
  const int wm = wave & 3, wk = wave >> 2;
  const int bid = blockIdx.x;
  const int xcd = bid & 7, idx = bid >> 3;
  const int kb = xcd & 3;
  const int m128 = (xcd >> 2) * 8 + (idx >> 2), nt = idx & 3;
  const unsigned voff = lane * 16;
  const unsigned lds0 = (unsigned)(uintptr_t)(LDSP char*)sm;
  const char* wsrc = ws + OFF_W2 + (size_t)(kb * 4 + nt) * 324 * 1024 + wave * 4608;
  const unsigned wdst = lds0 + wave * 4608;
  const char* h0 = ws + OFF_H2 + ((size_t)(m128 * 8 + wm * 2) * 144 + kb * 36 + wk) * 1024;
  const char* h1 = h0 + 144 * 1024;
  u32x4 H[36];
#define LDH(u) { ld_frag<0>(H[(u) * 4], h0 + (u) * 4096, voff); ld_frag<0>(H[(u) * 4 + 1], h1 + (u) * 4096, voff); \
                 ld_frag<2048>(H[(u) * 4 + 2], h0 + (u) * 4096, voff); ld_frag<2048>(H[(u) * 4 + 3], h1 + (u) * 4096, voff); }
  dma_unit(wsrc, voff, wdst);                       LDH(0)
  dma_unit(wsrc + UNIT, voff, wdst + UNIT);         LDH(1)
  dma_unit(wsrc + 2 * UNIT, voff, wdst + 2 * UNIT); LDH(2)
  dma_unit(wsrc + 3 * UNIT, voff, wdst + 3 * UNIT); LDH(3)
  f32x4 acc[2][9];
#pragma unroll
  for (int i = 0; i < 2; ++i)
#pragma unroll
    for (int dt = 0; dt < 9; ++dt) acc[i][dt] = (f32x4){0.f, 0.f, 0.f, 0.f};
#define PINH(u) PIN4(H[(u) * 4], H[(u) * 4 + 1], H[(u) * 4 + 2], H[(u) * 4 + 3])
  WAIT_VM(27); PINH(0); BARRIER();
  k4_unit<0>(sm, lane, wk, H, acc);
  WAIT_VM(18); PINH(1); BARRIER();
  dma_unit(wsrc + 4 * UNIT, voff, wdst); LDH(4)
  k4_unit<1>(sm, lane, wk, H, acc);
  WAIT_VM(18); PINH(2); BARRIER();
  dma_unit(wsrc + 5 * UNIT, voff, wdst + UNIT); LDH(5)
  k4_unit<2>(sm, lane, wk, H, acc);
  WAIT_VM(18); PINH(3); BARRIER();
  dma_unit(wsrc + 6 * UNIT, voff, wdst + 2 * UNIT); LDH(6)
  k4_unit<3>(sm, lane, wk, H, acc);
  WAIT_VM(18); PINH(4); BARRIER();
  dma_unit(wsrc + 7 * UNIT, voff, wdst + 3 * UNIT); LDH(7)
  k4_unit<4>(sm, lane, wk, H, acc);
  WAIT_VM(18); PINH(5); BARRIER();
  dma_unit(wsrc + 8 * UNIT, voff, wdst); LDH(8)
  k4_unit<5>(sm, lane, wk, H, acc);
  WAIT_VM(18); PINH(6); BARRIER();
  k4_unit<6>(sm, lane, wk, H, acc);
  WAIT_VM(9); PINH(7); BARRIER();
  k4_unit<7>(sm, lane, wk, H, acc);
  WAIT_VM(0); PINH(8); BARRIER();
  k4_unit<8>(sm, lane, wk, H, acc);
  f32x4 mine[9];
  {
    char* xb = sm + UNIT;
    char* wp = xb + wave * 9216 + lane * 16;
    if (wk == 0) {
#pragma unroll
      for (int dt = 0; dt < 9; ++dt) { *(f32x4*)(wp + dt * 1024) = acc[1][dt]; mine[dt] = acc[0][dt]; }
    } else {
#pragma unroll
      for (int dt = 0; dt < 9; ++dt) { *(f32x4*)(wp + dt * 1024) = acc[0][dt]; mine[dt] = acc[1][dt]; }
    }
    BARRIER();
    const char* rp = xb + (wave ^ 4) * 9216 + lane * 16;
#pragma unroll
    for (int dt = 0; dt < 9; ++dt) mine[dt] += *(const f32x4*)(rp + dt * 1024);
  }
  const int tile = m128 * 4 + nt;
  __amdgpu_buffer_rsrc_t prs = __builtin_amdgcn_make_buffer_rsrc((void*)(ws + OFF_PART + (size_t)tile * 4 * 73728), 0, 4 * 73728, 0x00020000);
  if (wm != kb) {
    const unsigned o = (unsigned)(kb * 73728 + (wm * 2 + wk) * 9216 + lane * 16);
#pragma unroll
    for (int dt = 0; dt < 9; ++dt)
      __builtin_amdgcn_raw_buffer_store_b128(__builtin_bit_cast(u32x4, mine[dt]), prs, o + dt * 1024, 0, 16);
  }
  WAIT_VM(0);
  BARRIER();
  GLBP unsigned* cnt = (GLBP unsigned*)(ws + OFF_CNT + tile * 128);
  if (tid == 0) __hip_atomic_fetch_add(cnt, 1u, __ATOMIC_RELAXED, __HIP_MEMORY_SCOPE_AGENT);
  if (wm != kb) return;
  const int m = m128 * 128 + wm * 32 + wk * 16 + (lane & 15);
  const int g = lane >> 4;
  u32x2 xr[9];
  f32x4 gbv[9];
#pragma unroll
  for (int dt = 0; dt < 9; ++dt) {
    const int dd = (nt * 9 + dt) * 16 + g * 4;
    xr[dt] = *(const u32x2*)(ws + OFF_XF + ((size_t)((m >> 5) * 36 + (dd >> 4)) * 64 + ((dd >> 3) & 1) * 32 + (m & 31)) * 16 + ((dd >> 2) & 1) * 8);
    gbv[dt] = *(const f32x4*)(ws + OFF_MOD + (5 * 576 + dd) * 4);
  }
  const float rsd = 1.f / hi16(((const unsigned*)(ws + OFF_ST2))[m]);
  const float rs2 = rsd * rsd;
  for (unsigned spins = 0; spins < (1u << 22); ++spins) {
    if ((unsigned)__builtin_amdgcn_readfirstlane(__hip_atomic_load(cnt, __ATOMIC_RELAXED, __HIP_MEMORY_SCOPE_AGENT)) >= 4u) break;
    __builtin_amdgcn_s_sleep(1);
  }
  __builtin_amdgcn_fence(__ATOMIC_ACQUIRE, "wavefront");
  u32x4 pp[3][9];
#pragma unroll
  for (int j = 0; j < 3; ++j) {
    const int src = (kb + 1 + j) & 3;
    const unsigned o = (unsigned)(src * 73728 + (wm * 2 + wk) * 9216 + lane * 16);
#pragma unroll
    for (int dt = 0; dt < 9; ++dt) pp[j][dt] = __builtin_amdgcn_raw_buffer_load_b128(prs, o + dt * 1024, 0, 16);
  }
#pragma unroll
  for (int dt = 0; dt < 9; ++dt) {
    const f32x4 r = (mine[dt] + __builtin_bit_cast(f32x4, pp[0][dt])) +
                    (__builtin_bit_cast(f32x4, pp[1][dt]) + __builtin_bit_cast(f32x4, pp[2][dt]));
    u32x2 o;
#pragma unroll
    for (int e = 0; e < 2; ++e) {
      float a = r[2 * e] * rs2, b = r[2 * e + 1] * rs2; bfr2(a, b);
      a *= gbv[dt][2 * e]; b *= gbv[dt][2 * e + 1]; bfr2(a, b);
      o[e] = pk2(lo16(xr[dt][e]) + a, hi16(xr[dt][e]) + b);
    }
    const int dd = (nt * 9 + dt) * 16 + g * 4;
    *(u32x2*)(out + (size_t)m * D_ + dd) = o;
  }
}

extern "C" void kernel_launch(void* const* d_in, const int* in_sizes, int n_in, void* d_out, int out_size,
                              void* d_ws, size_t ws_size, hipStream_t stream) {
  const u16* A_q = (const u16*)d_in[0];
  const u16* A_k = (const u16*)d_in[1];
  const u16* A_v = (const u16*)d_in[2];
  const u16* A_sh = (const u16*)d_in[3];
  const u16* A_sk = (const u16*)d_in[4];
  const u16* A_D = (const u16*)d_in[5];
  const u16* Wg = (const u16*)d_in[7];
  const u16* Wu = (const u16*)d_in[8];
  const u16* Wd = (const u16*)d_in[9];
  const u16* cv = (const u16*)d_in[10];
  const u16* wm = (const u16*)d_in[11];
  char* ws = (char*)d_ws;
  (void)hipFuncSetAttribute((const void*)k1_conv, hipFuncAttributeMaxDynamicSharedMemorySize, K1_LDS);
  (void)hipFuncSetAttribute((const void*)k3_gemm1, hipFuncAttributeMaxDynamicSharedMemorySize, K3_LDS);
  (void)hipFuncSetAttribute((const void*)k4_gemm2, hipFuncAttributeMaxDynamicSharedMemorySize, K4_LDS);
  k0_prep<<<K0_GRID, 256, 0, stream>>>(A_q, A_k, A_v, A_sh, cv, wm, Wd, ws);
  k1_conv<<<K1_CONV + K1_W, 512, K1_LDS, stream>>>(A_sk, A_D, Wg, Wu, ws);
  k3_gemm1<<<256, 512, K3_LDS, stream>>>(Wg, Wu, ws);
  k4_gemm2<<<256, 512, K4_LDS, stream>>>(ws, (u16*)d_out);
}
```

```cpp
#include <hip/hip_runtime.h>
#include <hip/hip_bf16.h>
#include <stdint.h>

typedef unsigned short u16;
typedef __attribute__((ext_vector_type(2))) __bf16 bf16x2;
typedef __attribute__((ext_vector_type(8))) __bf16 bf16x8;
typedef __attribute__((ext_vector_type(2))) float f32x2;
typedef __attribute__((ext_vector_type(4))) float f32x4;
typedef __attribute__((ext_vector_type(16))) float f32x16;
typedef __attribute__((ext_vector_type(4))) unsigned u32x4;
typedef __attribute__((ext_vector_type(2))) unsigned u32x2;
typedef __attribute__((ext_vector_type(4))) short s16x4;
#define LDSP __attribute__((address_space(3)))
#define GLBP __attribute__((address_space(1)))

constexpr int S_ = 2048, D_ = 576, F_ = 4608;

constexpr size_t OFF_CNT = 0;
constexpr size_t OFF_MOD = 8192;
constexpr size_t OFF_ST1 = OFF_MOD + 16384;
constexpr size_t OFF_ST2 = OFF_ST1 + 8192;
constexpr size_t OFF_CC  = OFF_ST2 + 8192;
constexpr size_t OFF_UT  = OFF_CC + 32 * 6912;
constexpr size_t OFF_XT  = OFF_UT + (size_t)D_ * S_ * 2;
constexpr size_t OFF_XF  = OFF_XT + (size_t)D_ * S_ * 2;
constexpr size_t OFF_XS  = OFF_XF + (size_t)D_ * S_ * 2;
constexpr size_t OFF_W2  = OFF_XS + (size_t)D_ * S_ * 2;
constexpr size_t OFF_H2  = OFF_W2 + (size_t)D_ * F_ * 2;
constexpr size_t OFF_PART = OFF_H2 + (size_t)S_ * F_ * 2;
constexpr int PART_TILE = 131072 + 16384;

__device__ __forceinline__ unsigned pk2(float a, float b) {
  f32x2 v = {a, b};
  return __builtin_bit_cast(unsigned, __builtin_convertvector(v, bf16x2));
}
__device__ __forceinline__ float lo16(unsigned u) { return __uint_as_float(u << 16); }
__device__ __forceinline__ float hi16(unsigned u) { return __uint_as_float(u & 0xffff0000u); }
__device__ __forceinline__ float bfr(float a) { return lo16(pk2(a, a)); }
__device__ __forceinline__ void bfr2(float& a, float& b) { unsigned u = pk2(a, b); a = lo16(u); b = hi16(u); }
__device__ __forceinline__ float bf2f(u16 x) { return __uint_as_float(((unsigned)x) << 16); }
__device__ __forceinline__ s16x4 tr_read(const char* p) {
  return __builtin_amdgcn_ds_read_tr16_b64_v4i16((LDSP s16x4*)p);
}
__device__ __forceinline__ u32x4 tr_read2(const char* p0, const char* p1) {
  s16x4 a = tr_read(p0), b = tr_read(p1);
  u32x2 x = __builtin_bit_cast(u32x2, a), y = __builtin_bit_cast(u32x2, b);
  u32x4 r = {x[0], x[1], y[0], y[1]};
  return r;
}

typedef LDSP const char* lptr;
__device__ __forceinline__ u32x4 tr_read2l(lptr p0, lptr p1) {
  s16x4 a = __builtin_amdgcn_ds_read_tr16_b64_v4i16((LDSP s16x4*)p0), b = __builtin_amdgcn_ds_read_tr16_b64_v4i16((LDSP s16x4*)p1);
  u32x2 x = __builtin_bit_cast(u32x2, a), y = __builtin_bit_cast(u32x2, b);
  u32x4 r = {x[0], x[1], y[0], y[1]};
  return r;
}

constexpr int K0_MOD = 108, K0_KV = 288, K0_ST = 64, K0_WD = 144;
constexpr int K0_GRID = K0_MOD + K0_KV + K0_ST + K0_WD;

__global__ __launch_bounds__(256) void k0_prep(const u16* __restrict__ xq, const u16* __restrict__ kk,
                                               const u16* __restrict__ vv, const u16* __restrict__ shift_k,
                                               const u16* __restrict__ cvec, const u16* __restrict__ w_mod,
                                               const u16* __restrict__ Wd, char* __restrict__ ws) {
  __shared__ __attribute__((aligned(16))) char sm[37888];
  const int tid = threadIdx.x, lane = tid & 63, wave = tid >> 6;
  int bid = blockIdx.x;
  if (bid < K0_MOD) {
    if (bid == 0 && tid < 64) *(unsigned*)(ws + OFF_CNT + tid * 128) = 0u;
    if (bid >= 1 && bid <= 8) ((unsigned*)(ws + OFF_ST2))[(bid - 1) * 256 + tid] = 0u;
    const int c4 = tid & 3, rg = tid >> 2;
    u32x4 w[9];
    float sc[9];
#pragma unroll
    for (int i = 0; i < 9; ++i) {
      const int row = rg + 64 * i;
      w[i] = *(const u32x4*)(w_mod + (size_t)row * 3456 + bid * 32 + c4 * 8);
      sc[i] = bf2f(cvec[row]);
    }
    float acc[8];
#pragma unroll
    for (int e = 0; e < 8; ++e) acc[e] = 0.f;
#pragma unroll
    for (int i = 0; i < 9; ++i) {
      const float s = bfr(sc[i] / (1.f + __expf(-sc[i])));
#pragma unroll
      for (int e = 0; e < 4; ++e) {
        acc[2 * e] += s * lo16(w[i][e]);
        acc[2 * e + 1] += s * hi16(w[i][e]);
      }
    }
#pragma unroll
    for (int e = 0; e < 8; ++e) {
#pragma unroll
      for (int o = 4; o < 64; o <<= 1) acc[e] += __shfl_xor(acc[e], o);
    }
    float* red = (float*)sm;
    if (lane < 4) {
#pragma unroll
      for (int e = 0; e < 8; ++e) red[wave * 32 + lane * 8 + e] = acc[e];
    }
    __syncthreads();
    if (tid < 32) {
      const float r = (red[tid] + red[32 + tid]) + (red[64 + tid] + red[96 + tid]);
      ((float*)(ws + OFF_MOD))[bid * 32 + tid] = bfr(r);
    }
    return;
  }
  bid -= K0_MOD;
  if (bid < K0_KV) {
    const int cb = bid % 9, tb = bid / 9;
    const int c8 = tid & 7, rp = tid >> 3;
    const int r0 = tb * 64 + rp * 2;
    const int ch0 = cb * 64 + c8 * 8;
    u32x4 kr[5], vr[2], xr[2], sw[4];
#pragma unroll
    for (int i = 0; i < 5; ++i) {
      const int r = r0 - 3 + i;
      const int rc = r < 0 ? 0 : r;
      kr[i] = *(const u32x4*)(kk + (size_t)rc * D_ + ch0);
      if (r < 0) kr[i] = (u32x4){0u, 0u, 0u, 0u};
    }
#pragma unroll
    for (int i = 0; i < 2; ++i) {
      vr[i] = *(const u32x4*)(vv + (size_t)(r0 + i) * D_ + ch0);
      xr[i] = *(const u32x4*)(xq + (size_t)(r0 + i) * D_ + ch0);
    }
#pragma unroll
    for (int i = 0; i < 4; ++i) sw[i] = *(const u32x4*)(shift_k + (size_t)ch0 * 4 + i * 8);
    char* tu = sm;
    char* tx = sm + 8192;
    const int q = rp >> 2, inner = rp & 3;
#pragma unroll
    for (int e = 0; e < 8; ++e) {
      const unsigned wa = sw[e >> 1][(e & 1) * 2], wb = sw[e >> 1][(e & 1) * 2 + 1];
      const float w0 = lo16(wa), w1 = hi16(wa), w2 = lo16(wb), w3 = hi16(wb);
      float kf[5];
#pragma unroll
      for (int i = 0; i < 5; ++i) kf[i] = (e & 1) ? hi16(kr[i][e >> 1]) : lo16(kr[i][e >> 1]);
      const float v0 = (e & 1) ? hi16(vr[0][e >> 1]) : lo16(vr[0][e >> 1]);
      const float v1 = (e & 1) ? hi16(vr[1][e >> 1]) : lo16(vr[1][e >> 1]);
      float a0 = kf[0] * w3, a1 = kf[1] * w3; bfr2(a0, a1);
      float t0 = kf[1] * w2, t1 = kf[2] * w2; bfr2(t0, t1);
      a0 += t0; a1 += t1; bfr2(a0, a1);
      t0 = kf[2] * w1; t1 = kf[3] * w1; bfr2(t0, t1);
      a0 += t0; a1 += t1; bfr2(a0, a1);
      t0 = kf[3] * w0; t1 = kf[4] * w0; bfr2(t0, t1);
      a0 += t0; a1 += t1; bfr2(a0, a1);
      const unsigned kvp = pk2(a0 * v0, a1 * v1);
      const unsigned x0 = xr[0][e >> 1], x1 = xr[1][e >> 1];
      const unsigned xp = (e & 1) ? ((x0 >> 16) | (x1 & 0xffff0000u)) : ((x0 & 0xffffu) | (x1 << 16));
      const int off = (c8 * 8 + e) * 128 + ((q ^ c8) * 16) + inner * 4;
      *(unsigned*)(tu + off) = kvp;
      *(unsigned*)(tx + off) = xp;
    }
    __syncthreads();
#pragma unroll
    for (int i = 0; i < 2; ++i) {
      const int p = tid + 256 * i, ch = p >> 3, qq = p & 7;
      const int off = ch * 128 + ((qq ^ (ch >> 3)) * 16);
      const size_t g = ((size_t)(cb * 64 + ch) * S_ + tb * 64 + qq * 8) * 2;
      *(u32x4*)(ws + OFF_UT + g) = *(const u32x4*)(tu + off);
      *(u32x4*)(ws + OFF_XT + g) = *(const u32x4*)(tx + off);
    }
    return;
  }
  bid -= K0_KV;
  if (bid < K0_ST) {
    const int row = bid * 32 + wave * 8 + (lane >> 3), c = lane & 7;
    u32x4 xr[9];
#pragma unroll
    for (int i = 0; i < 9; ++i) xr[i] = *(const u32x4*)(xq + (size_t)row * D_ + (c + 8 * i) * 8);
    float s = 0.f;
#pragma unroll
    for (int i = 0; i < 9; ++i)
#pragma unroll
      for (int e = 0; e < 4; ++e) s += lo16(xr[i][e]) + hi16(xr[i][e]);
    s += __shfl_xor(s, 1); s += __shfl_xor(s, 2); s += __shfl_xor(s, 4);
    const float mean = s * (1.f / 576.f);
    float ss = 0.f;
#pragma unroll
    for (int i = 0; i < 9; ++i)
#pragma unroll
      for (int e = 0; e < 4; ++e) {
        const float a = lo16(xr[i][e]) - mean, b = hi16(xr[i][e]) - mean;
        ss += a * a + b * b;
      }
    ss += __shfl_xor(ss, 1); ss += __shfl_xor(ss, 2); ss += __shfl_xor(ss, 4);
    const float var = bfr(ss * (1.f / 576.f));
    const float sd = sqrtf(bfr(var + 1e-5f));
    if (c == 0) ((unsigned*)(ws + OFF_ST1))[row] = pk2(mean, sd);
    return;
  }
  bid -= K0_ST;
  {
    const int T = bid;
    const char* src = (const char*)Wd + (size_t)T * 32 * 1152;
    u32x4 w[9];
#pragma unroll
    for (int i = 0; i < 9; ++i) w[i] = *(const u32x4*)(src + (tid + 256 * i) * 16);
#pragma unroll
    for (int i = 0; i < 9; ++i) {
      const int p = tid + 256 * i, row = p / 72, cc = p % 72;
      *(u32x4*)(sm + row * 1184 + cc * 16) = w[i];
    }
    __syncthreads();
    const int g = lane >> 4, q = (lane & 15) >> 2, p = lane & 3;
    const char* base = sm + (4 * g + q) * 1184 + (wave * 144 + 4 * p) * 2;
    const int kb = T / 36, Tl = T % 36;
    char* dst = ws + OFF_W2 + (size_t)(kb * 4 + wave) * 324 * 1024 + lane * 16;
#pragma unroll
    for (int i = 0; i < 9; ++i) {
      const u32x4 r = tr_read2(base + i * 32, base + i * 32 + 16 * 1184);
      const int o = ((i >> 1) - kb) & 3;
      const int pos = i == 8 ? 6 : (o ? 2 * (o - 1) + (i & 1) : 7 + (i & 1));
      *(u32x4*)(dst + (Tl < 24 ? Tl * 9 + pos : 216 + pos * 12 + Tl - 24) * 1024) = r;
    }
  }
}

constexpr int K1_CONV = 144, K1_W = 108;
constexpr int KC_STRIDE = 4416;
constexpr int CH_BYTES = 4 * KC_STRIDE + 4672;
constexpr int K1_XCH = 4 * CH_BYTES;
constexpr int K1_X1 = K1_XCH + 32768;
constexpr int K1_LDS = K1_X1 + 32768;

__global__ __launch_bounds__(512, 2) void k1_conv(const u16* __restrict__ ssm_k, const u16* __restrict__ ssm_D,
                                                  const u16* __restrict__ Wg, const u16* __restrict__ Wu,
                                                  char* __restrict__ ws) {
  extern __shared__ __attribute__((aligned(16))) char sm[];
  const int tid = threadIdx.x, lane = tid & 63, wave = tid >> 6;
  const float* mod = (const float*)(ws + OFF_MOD);
  int bid = blockIdx.x;
  if (bid < K1_CONV) {
    const int cg = (bid & 7) * 18 + (bid >> 3);
    const int cl = wave >> 1, w2 = wave & 1;
    const int d = cg * 4 + cl;
    char* chb = sm + cl * CH_BYTES;
    char* ub = chb + 4 * KC_STRIDE;
    const int t128 = tid & 127;
    const char* krow = (const char*)ssm_k + (size_t)d * 4096;
    const char* urow = ws + OFF_UT + (size_t)d * 4096;
    const char* xrow = ws + OFF_XT + (size_t)d * 4096;
    u32x4 ka[2], ua[2];
    u32x2 kb[2];
#pragma unroll
    for (int i = 0; i < 2; ++i) {
      const int c = t128 + 128 * i;
      ka[i] = *(const u32x4*)(krow + c * 16);
      const int cn = c < 255 ? c + 1 : 255;
      kb[i] = *(const u32x2*)(krow + cn * 16);
      if (c == 255) kb[i] = (u32x2){0u, 0u};
      ua[i] = *(const u32x4*)(urow + c * 16);
    }
    const int li = lane & 15, g = lane >> 4;
    u32x2 xv[4];
    u32x4 st[4];
#pragma unroll
    for (int i = 0; i < 4; ++i) {
      const int s = 256 * (4 * w2 + i) + 16 * li + 4 * g;
      xv[i] = *(const u32x2*)(xrow + s * 2);
      st[i] = *(const u32x4*)(ws + OFF_ST1 + s * 4);
    }
    const float sa = mod[d], c1a = bfr(1.f + mod[576 + d]), ga = mod[1152 + d];
    const float Dd = bf2f(ssm_D[d]);
    const float c1b = bfr(1.f + mod[4 * 576 + d]);
    {
      const u32x4 z = {0u, 0u, 0u, 0u};
      if (t128 < 32) *(u32x4*)(ub + t128 * 16) = z;
      else if (t128 < 32 + 12) {
        const int i = t128 - 32;
        *(u32x4*)(chb + (i / 3) * KC_STRIDE + (i % 3) * 16) = z;
      } else if (t128 < 32 + 12 + 16) {
        const int i = t128 - 44;
        *(u32x4*)(chb + (i >> 2) * KC_STRIDE + 64 + 4096 + (i & 3) * 16) = z;
      } else if (t128 < 64) {
        *(u32x4*)(ub + 4608 + (t128 - 60) * 16) = z;
      }
    }
#pragma unroll
    for (int i = 0; i < 2; ++i) {
      const int c = t128 + 128 * i;
      const unsigned w0 = ka[i][0], w1 = ka[i][1], w2_ = ka[i][2], w3 = ka[i][3], w4 = kb[i][0], w5 = kb[i][1];
      const unsigned a0 = __builtin_amdgcn_alignbit(w1, w0, 16), a1 = __builtin_amdgcn_alignbit(w2_, w1, 16),
                     a2 = __builtin_amdgcn_alignbit(w3, w2_, 16), a3 = __builtin_amdgcn_alignbit(w4, w3, 16),
                     a4 = __builtin_amdgcn_alignbit(w5, w4, 16);
      char* p = chb + 64 + c * 16;
      *(u32x4*)(p) = ka[i];
      *(u32x4*)(p + KC_STRIDE) = (u32x4){a0, a1, a2, a3};
      *(u32x4*)(p + 2 * KC_STRIDE) = (u32x4){w1, w2_, w3, w4};
      *(u32x4*)(p + 3 * KC_STRIDE) = (u32x4){a1, a2, a3, a4};
      *(u32x4*)(ub + 512 + c * 16) = ua[i];
      if (c == 0) {
        *(u32x4*)(p - 16) = (u32x4){0u, 0u, 0u, 0u};
        *(u32x4*)(p - 16 + KC_STRIDE) = (u32x4){0u, 0u, 0u, w0 << 16};
        *(u32x4*)(p - 16 + 2 * KC_STRIDE) = (u32x4){0u, 0u, 0u, w0};
        *(u32x4*)(p - 16 + 3 * KC_STRIDE) = (u32x4){0u, 0u, w0 << 16, a0};
      }
    }
    __syncthreads();
    const int q = li >> 2, p4 = li & 3;
    const char* abase = chb + ((4 - q) & 3) * KC_STRIDE + 2 * (32 - 8 * g - (q ? 4 : 0) + 4 * p4) - 8;
    const char* bbase = ub + 2 * (256 + 16 * li + 8 * g);
    f32x4 acc[8];
#pragma unroll
    for (int b = 0; b < 8; ++b) acc[b] = (f32x4){0.f, 0.f, 0.f, 0.f};
#pragma unroll 1
    for (int it = 0; it < 4; ++it) {
      const int r = 2 * it + 1 + w2;
      const char* ap = abase + 64 * r;
      const char* bp = bbase - 64 * r;
      bf16x8 A[8], B[8];
#pragma unroll
      for (int i = 0; i < 8; ++i) {
        A[i] = __builtin_bit_cast(bf16x8, tr_read2(ap + 512 * i + 8, ap + 512 * i));
        B[i] = *(const bf16x8*)(bp + 512 * i);
      }
#pragma unroll
      for (int b = 0; b < 8; ++b)
#pragma unroll
        for (int qq = 0; qq <= b; ++qq)
          acc[b] = __builtin_amdgcn_mfma_f32_16x16x32_bf16(A[qq], B[b - qq], acc[b], 0, 0, 0);
    }
    char* xch = sm + K1_XCH;
    f32x4 fin[4];
    if (w2 == 0) {
#pragma unroll
      for (int i = 0; i < 4; ++i) { *(f32x4*)(xch + wave * 4096 + i * 1024 + lane * 16) = acc[4 + i]; fin[i] = acc[i]; }
    } else {
#pragma unroll
      for (int i = 0; i < 4; ++i) { *(f32x4*)(xch + wave * 4096 + i * 1024 + lane * 16) = acc[i]; fin[i] = acc[4 + i]; }
    }
    {
      const bf16x8 A0 = __builtin_bit_cast(bf16x8, tr_read2(abase + 8, abase));
#pragma unroll
      for (int i = 0; i < 4; ++i) {
        const bf16x8 Bd = *(const bf16x8*)(bbase + 512 * (4 * w2 + i));
        fin[i] = __builtin_amdgcn_mfma_f32_16x16x32_bf16(A0, Bd, fin[i], 0, 0, 0);
      }
    }
    __syncthreads();
    char* x1s = sm + K1_X1;
#pragma unroll
    for (int i = 0; i < 4; ++i) {
      const f32x4 o = *(const f32x4*)(xch + (wave ^ 1) * 4096 + i * 1024 + lane * 16);
      const int s = 256 * (4 * w2 + i) + 16 * li + 4 * g;
      const u32x2 uu = *(const u32x2*)(ub + 512 + s * 2);
#pragma unroll
      for (int e = 0; e < 2; ++e) {
        const float x0 = lo16(xv[i][e]), x1 = hi16(xv[i][e]);
        const unsigned s0 = st[i][2 * e], s1 = st[i][2 * e + 1];
        float h0 = x0 - lo16(s0), h1 = x1 - lo16(s1); bfr2(h0, h1);
        h0 = h0 * __builtin_amdgcn_rcpf(hi16(s0)); h1 = h1 * __builtin_amdgcn_rcpf(hi16(s1)); bfr2(h0, h1);
        h0 *= c1a; h1 *= c1a; bfr2(h0, h1);
        h0 += sa; h1 += sa; bfr2(h0, h1);
        float y0 = fin[i][2 * e] + o[2 * e], y1 = fin[i][2 * e + 1] + o[2 * e + 1]; bfr2(y0, y1);
        float t0 = lo16(uu[e]) * Dd, t1 = hi16(uu[e]) * Dd; bfr2(t0, t1);
        y0 += t0; y1 += t1; bfr2(y0, y1);
        y0 *= h0; y1 *= h1; bfr2(y0, y1);
        y0 *= ga; y1 *= ga; bfr2(y0, y1);
        const unsigned r = pk2(x0 + y0, x1 + y1);
        *(u16*)(x1s + (s + 2 * e) * 8 + cl * 2) = (u16)(r & 0xffffu);
        *(u16*)(x1s + (s + 2 * e + 1) * 8 + cl * 2) = (u16)(r >> 16);
        const unsigned r2 = pk2(lo16(r) * c1b, hi16(r) * c1b);
        *(u16*)(x1s + 16384 + (s + 2 * e) * 8 + cl * 2) = (u16)(r2 & 0xffffu);
        *(u16*)(x1s + 16384 + (s + 2 * e + 1) * 8 + cl * 2) = (u16)(r2 >> 16);
      }
    }
    __syncthreads();
    const int d0 = cg * 4;
    char* xf = ws + OFF_XF + (d0 >> 5) * 1024 + ((d0 >> 2) & 3) * 256 + ((d0 >> 4) & 1) * 8;
#pragma unroll
    for (int i = 0; i < 4; ++i) {
      const int s = tid + 512 * i;
      *(u32x2*)(xf + (size_t)(s >> 4) * 18432 + (s & 15) * 16) = *(const u32x2*)(x1s + s * 8);
      *(u32x2*)(xf + (OFF_XS - OFF_XF) + (size_t)(s >> 4) * 18432 + (s & 15) * 16) = *(const u32x2*)(x1s + 16384 + s * 8);
    }
    return;
  }
  bid -= K1_CONV;
  {
    const int cbk = bid % 36, kt = bid / 36;
    const int mat = cbk / 18, cblk = cbk % 18;
    const u16* W = mat ? Wu : Wg;
    const int p = tid & 31, rg = tid >> 5;
    u32x4 w[12];
    float cs[12], sb[12];
#pragma unroll
    for (int i = 0; i < 12; ++i) {
      const int k = kt * 192 + rg + 16 * i;
      w[i] = *(const u32x4*)(W + (size_t)k * F_ + cblk * 256 + p * 8);
      cs[i] = mod[4 * 576 + k];
      sb[i] = mod[3 * 576 + k];
    }
    float a1[8], a2[8];
#pragma unroll
    for (int e = 0; e < 8; ++e) { a1[e] = 0.f; a2[e] = 0.f; }
#pragma unroll
    for (int i = 0; i < 12; ++i) {
      const float c1 = bfr(1.f + cs[i]);
#pragma unroll
      for (int e = 0; e < 4; ++e) {
        const float l = lo16(w[i][e]), h = hi16(w[i][e]);
        a1[2 * e] += c1 * l; a1[2 * e + 1] += c1 * h;
        a2[2 * e] += sb[i] * l; a2[2 * e + 1] += sb[i] * h;
      }
    }
    float* red = (float*)(sm + 110592);
#pragma unroll
    for (int e = 0; e < 8; ++e) { a1[e] += __shfl_xor(a1[e], 32); a2[e] += __shfl_xor(a2[e], 32); }
    if (lane < 32) {
      *(f32x4*)(red + wave * 512 + p * 8) = (f32x4){a1[0], a1[1], a1[2], a1[3]};
      *(f32x4*)(red + wave * 512 + p * 8 + 4) = (f32x4){a1[4], a1[5], a1[6], a1[7]};
    } else {
      *(f32x4*)(red + wave * 512 + 256 + p * 8) = (f32x4){a2[0], a2[1], a2[2], a2[3]};
      *(f32x4*)(red + wave * 512 + 256 + p * 8 + 4) = (f32x4){a2[4], a2[5], a2[6], a2[7]};
    }
    __syncthreads();
    {
      float r = 0.f;
#pragma unroll
      for (int i = 0; i < 8; ++i) r += red[i * 512 + tid];
      const int which = tid >> 8, col = tid & 255;
      const int ft = cblk * 16 + (col >> 4);
      ((float*)(ws + OFF_CC))[(ft / 9) * 1728 + kt * 576 + which * 288 + (ft % 9) * 32 + mat * 16 + (col & 15)] = r;
    }
  }
}

constexpr int UNIT = 36864;

__device__ __forceinline__ void dma_unit(const char* gbase  , unsigned voff, unsigned lds_dst  ) {
  unsigned keep;
  asm volatile(
      "s_mov_b32 %0, m0\n\t"
      "s_mov_b32 m0, %3\n\t"
      "s_mov_b32 exec_hi, 0\n\t"
      "global_load_lds_dwordx4 %1, %2\n\t"
      "s_mov_b32 exec_hi, -1\n\t"
      "global_load_lds_dwordx4 %1, %2 offset:512\n\t"
      "global_load_lds_dwordx4 %1, %2 offset:1536\n\t"
      "global_load_lds_dwordx4 %1, %2 offset:2560\n\t"
      "global_load_lds_dwordx4 %1, %2 offset:3584\n\t"
      "s_mov_b32 m0, %0"
      : "=&s"(keep)
      : "v"(voff), "s"(gbase), "s"(lds_dst)
      : "memory");
}
template <int OFF>
__device__ __forceinline__ void ld_frag(u32x4& dst, const char* gbase  , unsigned voff) {
  asm volatile("global_load_dwordx4 %0, %1, %2 offset:%3" : "=v"(dst) : "v"(voff), "s"(gbase), "i"(OFF) : "memory");
}
#define TS_STEP(i)
#define H2_AUX 16
#define BARRIER() { asm volatile("s_waitcnt lgkmcnt(0)" ::: "memory"); __builtin_amdgcn_s_barrier(); asm volatile("" ::: "memory"); }
#define WAIT_VM(n) asm volatile("s_waitcnt vmcnt(" #n ")" ::: "memory")
#define PIN4(a, b, c, d) asm volatile("" : "+v"(a), "+v"(b), "+v"(c), "+v"(d))

constexpr int K3_UP = 18432;
constexpr int K3_CCRAW = 4 * UNIT;
constexpr int K3_CC = K3_CCRAW + 6912;
constexpr int K3_LDS = K3_CC + 4608;
constexpr int K3_D = 6;

__device__ __forceinline__ void dma_rows(const char* gbase  , const unsigned (&vo)[5], unsigned lds_dst  ) {
  asm volatile(
      "s_mov_b32 m0, %6\n\t"
      "s_mov_b32 exec_hi, 0\n\t"
      "global_load_lds_dwordx4 %0, %5\n\t"
      "s_mov_b32 exec_hi, -1\n\t"
      "global_load_lds_dwordx4 %1, %5 offset:512\n\t"
      "global_load_lds_dwordx4 %2, %5 offset:1536\n\t"
      "global_load_lds_dwordx4 %3, %5 offset:2560\n\t"
      "global_load_lds_dwordx4 %4, %5 offset:3584"
      :
      : "v"(vo[0]), "v"(vo[1]), "v"(vo[2]), "v"(vo[3]), "v"(vo[4]), "s"(gbase), "s"(lds_dst)
      : "memory");
}
#define SB0() __builtin_amdgcn_sched_barrier(0)
#define XBAR() { SB0(); asm volatile("s_barrier" ::: "memory"); SB0(); }

template <int V> struct IC { static constexpr int value = V; };
template <int I0, int I1, class F>
__device__ __forceinline__ void static_for(F&& f) {
  if constexpr (I0 < I1) { f(IC<I0>{}); static_for<I0 + 1, I1>(f); }
}
template <int J>
__device__ __forceinline__ void dma_piece(const char* gbase  , unsigned vo, unsigned lds_dst  ) {
  if constexpr (J == 0)
    asm volatile("s_mov_b32 m0, %2\n\ts_mov_b32 exec_hi, 0\n\tglobal_load_lds_dwordx4 %0, %1\n\ts_mov_b32 exec_hi, -1"
                 :: "v"(vo), "s"(gbase), "s"(lds_dst) : "memory");
  else
    asm volatile("s_mov_b32 m0, %2\n\tglobal_load_lds_dwordx4 %0, %1 offset:%3"
                 :: "v"(vo), "s"(gbase), "s"(lds_dst), "i"(1024 * J - 512) : "memory");
}
__device__ __forceinline__ f32x4 mma16(u32x4 a, u32x4 b, f32x4 c) {
  return __builtin_amdgcn_mfma_f32_16x16x32_bf16(__builtin_bit_cast(bf16x8, a), __builtin_bit_cast(bf16x8, b), c, 0, 0, 0);
}
template <bool Z>
__device__ __forceinline__ void mma16t(f32x4& c, u32x4 a, u32x4 b) {
  if constexpr (Z) asm volatile("v_mfma_f32_16x16x32_bf16 %0, %1, %2, 0" : "=v"(c) : "v"(a), "v"(b));
  else asm volatile("v_mfma_f32_16x16x32_bf16 %0, %1, %2, %0" : "+v"(c) : "v"(a), "v"(b));
}
#define MFMA_DRAIN() { asm volatile("s_nop 7\n\ts_nop 7" ::: "memory"); SB0(); }
__device__ __forceinline__ float relu(float a) { return __int_as_float(max(__float_as_int(a), 0)); }
__device__ __forceinline__ u32x2 reglu(f32x4 g, f32x4 u) {
  float p[4];
#pragma unroll
  for (int i = 0; i < 4; ++i) asm("v_mul_f32 %0, %1, %2" : "=v"(p[i]) : "v"(relu(g[i])), "v"(u[i]));
  return (u32x2){pk2(p[0], p[1]), pk2(p[2], p[3])};
}

enum { L_NONE = 0, L_X, L_W, L_CC, L_POLL };
struct K3E {
  int pos, u, k32, tm, off;
  int epi;
  int lk, la, lb;
};
constexpr int K3_N = 342;
struct K3T { K3E e[K3_N]; };
constexpr K3T k3_table() {
  K3T t{};
  int n = 0;
  for (int i = 0; i < 324; ++i) {
    const int u = i / 36, r = i % 36;
    K3E e{};
    e.pos = i; e.u = u; e.epi = -1;
    if (u < 6) {
      e.k32 = 2 * u + r / 18; e.tm = r % 18;
      e.off = (e.tm & 1) * K3_UP + (r / 18) * 9216 + (e.tm >> 1) * 32;
    } else {
      e.k32 = 12 + (r % 12) / 2; e.tm = ((u - 6) * 3 + r / 12) * 2 + (r & 1);
      e.off = (r & 1) * K3_UP + ((r % 12) / 2) * 3072 + (r / 12) * 32;
      if (i > 228 && r % 12 == 2) e.epi = (e.tm >> 1) - 1;
    }
    if (u == 0) {
      if (r < 5) { e.lk = L_W; e.la = 1; e.lb = r; }
      else if (r == 5) e.lk = L_CC;
      else if (r >= 8 && r <= 20 && r % 3 == 2) { e.lk = L_W; e.la = 2; e.lb = (r - 8) / 3; }
      else if (r >= 23 && r <= 32 && r % 3 == 2) { e.lk = L_X; e.la = 8 + (r - 23) / 3; }
    } else if (u == 5) {
      if (r < 4) { e.lk = L_X; e.la = 32 + r; }
      else if (r >= 6 && r <= 18 && r % 3 == 0) { e.lk = L_W; e.la = 7; e.lb = (r - 6) / 3; }
      else if (r == 21) e.lk = L_POLL;
    } else if (u <= 6) {
      if (r <= 12 && r % 3 == 0) { e.lk = L_W; e.la = u + 2; e.lb = r / 3; }
      else if (u < 4 && r >= 15 && r <= 24 && r % 3 == 0) { e.lk = L_X; e.la = 8 + 4 * u + (r - 15) / 3; }
      else if (u == 4 && r >= 15 && r <= 29 && (r & 1)) { e.lk = L_X; e.la = 24 + (r - 15) / 2; }
    }
    t.e[n++] = e;
    if (u >= 6 && r % 12 == 11) {
      K3E c{};
      c.pos = -1; c.epi = -1;
      c.tm = e.tm - 1; c.off = c.tm * 256; t.e[n++] = c;
      c.tm = e.tm; c.off = c.tm * 256; t.e[n++] = c;
    }
  }
  return t;
}
constexpr K3T K3TAB = k3_table();
constexpr int k3_younger(int J, int lk, int la, int lb) {
  int n = 0;
  for (int j = 0; j < J; ++j) {
    const K3E& e = K3TAB.e[j];
    if (e.epi >= 0) ++n;
    if (e.lk) n += e.lk == L_POLL ? 2 : 1;
    if (e.lk == lk && e.la == la && e.lb == lb) n = 0;
  }
  return n;
}
template <int N> __device__ __forceinline__ void wait_vm() { asm volatile("s_waitcnt vmcnt(%0)" :: "n"(N) : "memory"); }

__global__ __launch_bounds__(512, 2) void k3_gemm1(const u16* __restrict__ Wg, const u16* __restrict__ Wu, char* __restrict__ ws) {
  extern __shared__ __attribute__((aligned(16))) char sm[];
  const int tid = threadIdx.x, lane = tid & 63;
  const int wave = __builtin_amdgcn_readfirstlane(tid >> 6);
  const int bid = blockIdx.x;
  const int xcd = bid & 7, idx = bid >> 3;
  const int m256 = (xcd >> 2) * 4 + (idx >> 3), sl = (xcd & 3) * 8 + (idx & 7);
  const int mt = m256 * 8 + wave;
  const unsigned voff = lane * 16;
  const unsigned lds0 = (unsigned)(uintptr_t)(LDSP char*)sm;
  const char* wA = (const char*)((wave & 4) ? Wu : Wg) + (size_t)(wave & 3) * 16 * 9216 + sl * 288;
  const char* wB = (const char*)((wave & 4) ? Wu : Wg) + (size_t)(384 + (wave & 3) * 48) * 9216 + sl * 288;
  const unsigned wdst = lds0 + wave * 4608;
  unsigned vo[5];
#pragma unroll
  for (int j = 0; j < 5; ++j) {
    const unsigned off = j ? 1024 * j - 512 : 0, c = off / 16 + lane;
    vo[j] = (c / 18) * 9216 + (c % 18) * 16 - off;
  }
  const char* xsrc = ws + OFF_XS + (size_t)mt * 36864;
  u32x4 X[36];
#define LDX(i) ld_frag<(((i) >> 1) & 1) * 1024>(X[i], xsrc + ((i) & 1) * 18432 + ((i) >> 2) * 2048, voff)
#define LDX4(i) { LDX(i); LDX((i) + 1); LDX((i) + 2); LDX((i) + 3); }
  dma_rows(wA, vo, wdst);
  LDX4(0)
  XBAR();
  LDX4(4)
  u32x4 sx[9];
  if (wave == 7) {
    const int r0 = m256 * 256 + sl * 8;
    const char* sb = ws + OFF_XF + (size_t)(r0 >> 4) * 18432 + (r0 & 15) * 16;
    const unsigned so = (lane >> 3) * 256 + (lane & 7) * 16;
#pragma unroll
    for (int i = 0; i < 9; ++i) ld_frag<0>(sx[i], sb + i * 2048, so);
    WAIT_VM(13);
  } else {
    WAIT_VM(4);
  }
  const char* csrc = ws + OFF_CC + (size_t)sl * 6912 + wave * 864;
  const unsigned cdst = lds0 + K3_CCRAW + wave * 864;
  f32x4 acc[36];
  __amdgpu_buffer_rsrc_t hrs = __builtin_amdgcn_make_buffer_rsrc((void*)(ws + OFF_H2 + (size_t)mt * 294912), 0, 294912, 0x00020000);
  const int rw = (lane >> 4) * 4 + ((lane & 15) >> 2);
  lptr bu = (lptr)sm;
  lptr cp = (lptr)sm + K3_CC + (lane & 15) * 16;
  u32x4 A[K3_D];
  unsigned stw[2];
  u32x4 bc[2];
  const char* stp = ws + OFF_ST2 + (size_t)mt * 128;
  auto rd = [&](auto jc, auto hc) -> u32x2 {
    constexpr int H = decltype(hc)::value;
    constexpr K3E E = K3TAB.e[decltype(jc)::value];
    if constexpr (E.pos >= 0) {
      constexpr int RS = E.u < 6 ? 288 : 96;
      if constexpr (E.pos % 36 == 0 && H == 0) { bu = (lptr)sm + (E.u & 3) * UNIT + rw * RS + (lane & 3) * 8; asm volatile("" : "+v"(bu)); }
      return __builtin_bit_cast(u32x2, __builtin_amdgcn_ds_read_tr16_b64_v4i16((LDSP s16x4*)(bu + E.off + H * 16 * RS)));
    } else {
      return *(LDSP const u32x2*)(cp + E.off + H * 8);
    }
  };
  auto epi = [&](auto tc) {
    constexpr int T = decltype(tc)::value, c = T * 4;
    const u32x2 h0 = reglu(acc[c], acc[c + 2]), h1 = reglu(acc[c + 1], acc[c + 3]);
    __builtin_amdgcn_raw_buffer_store_b128((u32x4){h0[0], h0[1], h1[0], h1[1]}, hrs, voff, (sl * 9 + T) * 1024, H2_AUX);
    SB0();
  };
#define POLL() asm volatile("global_load_dword %0, %2, %3 sc1\n\tglobal_load_dword %1, %2, %3 offset:64 sc1" \
                            : "=&v"(stw[0]), "=&v"(stw[1]) : "v"((lane & 15) * 4), "s"(stp) : "memory")
  PIN4(X[0], X[1], X[2], X[3]);
  XBAR();
  static_for<0, K3_D>([&](auto jc) {
    const u32x2 n0 = rd(jc, IC<0>{}), n1 = rd(jc, IC<1>{});
    A[decltype(jc)::value] = (u32x4){n0[0], n0[1], n1[0], n1[1]};
  });
  if (wave == 7) {
    WAIT_VM(0);
    PIN4(sx[0], sx[1], sx[2], sx[3]); PIN4(sx[4], sx[5], sx[6], sx[7]); asm volatile("" : "+v"(sx[8]));
    const bf16x2 ones = __builtin_bit_cast(bf16x2, 0x3f803f80u);
    float s1[2] = {0.f, 0.f}, s2[2] = {0.f, 0.f};
#pragma unroll
    for (int i = 0; i < 9; ++i)
#pragma unroll
      for (int e = 0; e < 4; ++e) {
        const unsigned xu = sx[i][e];
        const bf16x2 xx = __builtin_bit_cast(bf16x2, xu);
        s1[e & 1] = __builtin_amdgcn_fdot2_f32_bf16(xx, ones, s1[e & 1], false);
        s2[e & 1] = __builtin_amdgcn_fdot2_f32_bf16(xx, xx, s2[e & 1], false);
      }
    float a = s1[0] + s1[1], b = s2[0] + s2[1];
#pragma unroll
    for (int o = 8; o < 64; o <<= 1) { a += __shfl_xor(a, o); b += __shfl_xor(b, o); }
    const float mean = a * (1.f / 576.f);
    const float var = fmaxf(b * (1.f / 576.f) - mean * mean, 0.f);
    const unsigned w = pk2(mean, sqrtf(bfr(bfr(var) + 1e-5f)));
    if (lane < 8)
      __hip_atomic_store((GLBP unsigned*)(ws + OFF_ST2) + m256 * 256 + sl * 8 + lane, w, __ATOMIC_RELAXED, __HIP_MEMORY_SCOPE_AGENT);
  }
  SB0();
  static_for<0, K3_N>([&](auto jc) {
    constexpr int J = decltype(jc)::value;
    constexpr K3E E = K3TAB.e[J];
    constexpr int I = E.pos;
    if constexpr (I >= 0 && I % 36 == 30 && I < 300) {
      wait_vm<k3_younger(J, I == 30 ? L_CC : L_W, I == 30 ? 0 : E.u + 1, I == 30 ? 0 : 4)>();
      XBAR();
      TS_STEP(E.u + 2)
    }
    if constexpr (I == 32) {
      if (tid < 288) {
        const float* rp = (const float*)(sm + K3_CCRAW) + tid;
        const float c1 = rp[0] + rp[576] + rp[1152], c2 = rp[288] + rp[864] + rp[1440];
        const float h1 = bfr(c1), r1 = c1 - h1, m1 = bfr(r1), h2 = bfr(c2), r2 = c2 - h2, m2 = bfr(r2);
        *(u32x4*)(sm + K3_CC + tid * 16) = (u32x4){pk2(h1, m1), pk2(r1 - m1, h2), pk2(m2, r2 - m2), 0u};
      }
      SB0();
    }
    if constexpr (I > 0 && (I <= 216 ? I % 36 == 0 : (I == 220 || I == 224))) {
      constexpr int x0 = E.k32 * 2;
      wait_vm<k3_younger(J, L_X, x0 + 3, 0)>();
      PIN4(X[x0], X[x0 + 1], X[x0 + 2], X[x0 + 3]);
      SB0();
    }
    if constexpr (I == 130) {
#pragma unroll
      for (int j = 0; j < 5; ++j) {
        const unsigned off = j ? 1024 * j - 512 : 0, c = off / 16 + lane;
        vo[j] = (c / 6) * 9216 + (c % 6) * 16 - off;
      }
      SB0();
    }
    if constexpr (I == 226) {
      wait_vm<k3_younger(J, L_POLL, 0, 0)>();
      asm volatile("" : "+v"(stw[0]), "+v"(stw[1]));
      for (unsigned spins = 0; __builtin_amdgcn_ballot_w64(stw[0] == 0u || stw[1] == 0u) != 0ull && spins < (1u << 20); ++spins) {
        __builtin_amdgcn_s_sleep(2);
        POLL(); WAIT_VM(0); asm volatile("" : "+v"(stw[0]), "+v"(stw[1]));
      }
#pragma unroll
      for (int m = 0; m < 2; ++m) {
        const unsigned w = lane < 16 ? (stw[m] ^ 0x8000u) : 0u;
        const unsigned nm = w & 0xffffu, sd = w >> 16;
        bc[m] = (u32x4){nm | (nm << 16), w, sd | (sd << 16), 0u};
      }
      asm volatile("" : "+v"(bc[0]), "+v"(bc[1]));
      SB0();
    }
    if constexpr (E.epi >= 0) epi(IC<E.epi>{});
    const u32x4 a = A[J % K3_D];
    constexpr bool Z = I >= 0 && I < 18;
    u32x2 n0, n1;
    if constexpr (I >= 0) mma16t<Z>(acc[2 * E.tm], a, X[E.k32 * 2]); else mma16t<false>(acc[2 * E.tm], a, bc[0]);
    if constexpr (J + K3_D < K3_N) n0 = rd(IC<J + K3_D>{}, IC<0>{});
    SB0();
    if constexpr (I >= 0) mma16t<Z>(acc[2 * E.tm + 1], a, X[E.k32 * 2 + 1]); else mma16t<false>(acc[2 * E.tm + 1], a, bc[1]);
    if constexpr (J + K3_D < K3_N) { n1 = rd(IC<J + K3_D>{}, IC<1>{}); A[J % K3_D] = (u32x4){n0[0], n0[1], n1[0], n1[1]}; }
    if constexpr (E.lk == L_X) LDX(E.la);
    if constexpr (E.lk == L_W)
      dma_piece<E.lb>(E.la < 6 ? wA + (size_t)E.la * 64 * 9216 : wB + (E.la - 6) * 96, vo[E.lb], wdst + (E.la & 3) * UNIT);
    if constexpr (E.lk == L_CC)
      asm volatile("s_mov_b32 m0, %2\n\ts_mov_b32 exec_hi, 0x3fffff\n\tglobal_load_lds_dwordx4 %0, %1\n\ts_mov_b32 exec_hi, -1"
                   :: "v"(voff), "s"(csrc), "s"(cdst) : "memory");
    if constexpr (E.lk == L_POLL) POLL();
    SB0();
  });
  MFMA_DRAIN();
  epi(IC<8>{});
}

constexpr int K4_LDS = 4 * UNIT + 16384;
constexpr int K4_N = 162, K4_D = 6;
enum { L_R = 5 };
struct K4E { int u, q, hb, b, off, lk, la, lb; };
struct K4T { K4E e[K4_N]; };
constexpr K4T k4_table() {
  K4T t{};
  for (int i = 0; i < K4_N; ++i) {
    const int u = i / 18, r = i % 18;
    K4E e{};
    e.u = u;
    e.q = u < 6 ? r % 9 : (u - 6) * 3 + r / 6;
    e.hb = u < 6 ? 2 * u + r / 9 : 12 + r % 6;
    e.b = e.q == 6 ? 2 : (e.q < 6 ? e.q & 1 : e.q - 7);
    const int pb = e.q == 6 ? 6 : e.q - e.b;
    e.off = u < 6 ? ((r / 9) * 18 + pb) * 1024 : ((u == 8 ? pb - 6 : pb) * 12 + 2 * (r % 6)) * 1024;
    if (u == 0) {
      if (r < 5) { e.lk = L_W; e.la = 2; e.lb = r; }
      else if (r < 9) { e.lk = L_X; e.la = 8 + r - 5; }
      else if (r < 14) { e.lk = L_W; e.la = 3; e.lb = r - 9; }
      else { e.lk = L_X; e.la = 12 + r - 14; }
    } else if (u <= 2) {
      if (r <= 8 && !(r & 1)) { e.lk = L_W; e.la = u + 3; e.lb = r / 2; }
      else if (r >= 10 && !(r & 1)) { e.lk = L_X; e.la = 12 + 4 * u + (r - 10) / 2; }
    } else if (u <= 5) {
      if (r <= 8 && !(r & 1)) { e.lk = L_W; e.la = u + 3; e.lb = r / 2; }
      else if (u < 5 && r >= 10 && r < 16) { e.lk = L_X; e.la = 24 + 6 * (u - 3) + r - 10; }
      else if (u == 5 && r >= 10 && r < 17) { e.lk = L_R; e.la = r - 10; }
    }
    t.e[i] = e;
  }
  return t;
}
constexpr K4T K4TAB = k4_table();
constexpr int k4_send(int i) { return i == 122 ? 0 : i == 134 ? 1 : i == 146 ? 2 : -1; }
constexpr int k4_younger(int J, int lk, int la, int lb, int pre) {
  int n = pre;
  for (int j = 0; j < J; ++j) {
    const K4E& e = K4TAB.e[j];
    if (k4_send(j) >= 0) ++n;
    if (e.lk) ++n;
    if (e.lk == lk && e.la == la && e.lb == lb) n = 0;
  }
  return n;
}
constexpr int k4_xoff(int j) { return j < 2 ? UNIT + j * 16384 : j == 2 ? 4 * UNIT : 2 * UNIT + (j - 3) * 16384; }
__device__ __forceinline__ f32x4 add4(f32x4 a, f32x4 b) {
  f32x4 r;
#pragma unroll
  for (int i = 0; i < 4; ++i) asm("v_add_f32 %0, %1, %2" : "=v"(r[i]) : "v"(a[i]), "v"(b[i]));
  return r;
}

__global__ __launch_bounds__(512, 2) void k4_gemm2(char* __restrict__ ws, u16* __restrict__ out) {
  extern __shared__ __attribute__((aligned(16))) char sm[];
  const int tid = threadIdx.x, lane = tid & 63;
  const int wave = __builtin_amdgcn_readfirstlane(tid >> 6);
  const int wm = wave & 3, wk = wave >> 2;
  const int bid = blockIdx.x;
  const int xcd = bid & 7, idx = bid >> 3;
  const int kb = xcd & 3;
  const int m128 = (xcd >> 2) * 8 + (idx >> 2), nt = idx & 3;
  const unsigned voff = lane * 16;
  const unsigned lds0 = (unsigned)(uintptr_t)(LDSP char*)sm;
  const char* wsrc = ws + OFF_W2 + (size_t)(kb * 4 + nt) * 324 * 1024 + wave * 4608;
  const unsigned wdst = lds0 + wave * 4608;
  const char* h0 = ws + OFF_H2 + ((size_t)(m128 * 4 + wm) * 288 + (kb * 36 + wk) * 2) * 1024;
  u32x4 H[36];
#define LDH(i) ld_frag<((i) & 1) * 1024>(H[i], h0 + ((i) >> 1) * 4096, voff)
  dma_unit(wsrc, voff, wdst);
  LDH(0); LDH(1); LDH(2); LDH(3);
  XBAR();
  dma_unit(wsrc + UNIT, voff, wdst + UNIT);
  LDH(4); LDH(5); LDH(6); LDH(7);
  f32x4 acc[18];
  u32x4 A[K4_D];
  lptr bu[3];
  LDSP char* xw = (LDSP char*)sm + wave * 2048 + lane * 16;
  lptr xr_ = (lptr)sm + (wave ^ 4) * 2048 + lane * 16;
  const int tile = m128 * 4 + nt;
  __amdgpu_buffer_rsrc_t prs = __builtin_amdgcn_make_buffer_rsrc((void*)(ws + OFF_PART + (size_t)tile * PART_TILE), 0, PART_TILE, 0x00020000);
  f32x4 oth[2], f8;
  const int r0 = m128 * 128 + wm * 32, dA = nt * 9 + 2 * kb + wk, d8 = nt * 9 + 8;
  const char* xfA = ws + OFF_XF + (size_t)(r0 >> 4) * 18432 + (dA >> 1) * 1024 + (dA & 1) * 8;
  const char* xf8 = ws + OFF_XF + (size_t)((r0 >> 4) + wk) * 18432 + (d8 >> 1) * 1024 + (d8 & 1) * 8;
  const char* gbp = ws + OFF_MOD + 5 * 576 * 4;
  const char* stp = ws + OFF_ST2 + r0 * 4;
  u32x2 xres[3];
  u32x4 gbv[2];
  unsigned stw[2];
  auto rd = [&](auto jc) {
    constexpr int J = decltype(jc)::value;
    constexpr K4E E = K4TAB.e[J];
    if constexpr (J % 18 == 0 && E.u != 7) {
      constexpr int ST = E.u < 6 ? 1024 : 12288;
      bu[2] = (lptr)sm + (E.u & 3) * UNIT + wk * (E.u < 6 ? 9216 : 1024) + lane * 16;
      bu[1] = bu[2] + wk * ST; bu[0] = bu[2] + (1 - wk) * ST;
      asm volatile("" : "+v"(bu[0]), "+v"(bu[1]), "+v"(bu[2]));
    }
    return *(LDSP const u32x4*)(bu[E.b] + E.off);
  };
  WAIT_VM(9);
  XBAR();
  static_for<0, K4_D>([&](auto jc) { A[decltype(jc)::value] = rd(jc); });
  SB0();
  static_for<0, K4_N>([&](auto jc) {
    constexpr int J = decltype(jc)::value;
    constexpr K4E E = K4TAB.e[J];
    constexpr int R = J - 108;
    if constexpr (J % 18 == 12 && J < 108) {
      wait_vm<k4_younger(J, L_W, E.u + 1, 4, 4)>();
      asm volatile("s_waitcnt lgkmcnt(0)" ::: "memory");
      XBAR();
      TS_STEP(E.u + 2)
    }
    if constexpr (R == 3) { wait_vm<k4_younger(J, L_W, 7, 4, 0)>(); XBAR(); }
    if constexpr (R == 7 || R == 19 || R == 31 || R == 49) {
      constexpr int j = R == 49 ? 4 : R / 12, q = E.q - 1;
      *(LDSP f32x4*)(xw + k4_xoff(j)) = acc[2 * q];
      *(LDSP f32x4*)(xw + k4_xoff(j) + 1024) = acc[2 * q + 1];
      SB0();
    }
    if constexpr (R == 43) {
      if (wk) { *(LDSP f32x4*)(xw + k4_xoff(3)) = acc[12]; f8 = acc[13]; } else { *(LDSP f32x4*)(xw + k4_xoff(3)) = acc[13]; f8 = acc[12]; }
      SB0();
    }
    if constexpr (R == 9 || R == 21 || R == 33 || R == 45 || R == 51) {
      if constexpr (R == 21) wait_vm<k4_younger(J, L_W, 8, 4, 0)>();
      asm volatile("s_waitcnt lgkmcnt(%0)" :: "n"(R < 48 ? 2 : 0) : "memory");
      XBAR();
      if constexpr (R == 9 || R == 33) { TS_STEP(R == 9 ? 8 : 9) }
      constexpr int j = R == 51 ? 4 : R / 12;
      oth[0] = *(LDSP const f32x4*)(xr_ + k4_xoff(j));
      if constexpr (j != 3) oth[1] = *(LDSP const f32x4*)(xr_ + k4_xoff(j) + 1024);
      SB0();
    }
    if constexpr (k4_send(J) >= 0) {
      constexpr int j = k4_send(J);
      const f32x4 s0 = add4(acc[4 * j + 2], oth[0]), s1 = add4(acc[4 * j + 3], oth[1]);
      __builtin_amdgcn_raw_buffer_store_b128((u32x4){pk2(s0[0], s0[1]), pk2(s0[2], s0[3]), pk2(s1[0], s1[1]), pk2(s1[2], s1[3])}, prs, voff,
                                             ((((kb + j + 1) & 3) * 4 + kb) * 8 + wave) * 1024, 16);
      SB0();
    }
    if constexpr (R == 47) {
      f8 = add4(f8, oth[0]);
      if (wm != kb)
        __builtin_amdgcn_raw_buffer_store_b64((u32x2){pk2(f8[0], f8[1]), pk2(f8[2], f8[3])}, prs, lane * 8, 131072 + ((wm * 4 + kb) * 2 + wk) * 512, 16);
      SB0();
    }
    constexpr bool first = E.u < 6 ? E.q == 0 : J < 114;
    if constexpr (first) {
      constexpr int hb = E.hb;
      if constexpr (hb >= 2) wait_vm<k4_younger(J, L_X, 2 * hb + 1, 0, hb == 2 ? 2 : 0)>();
      asm volatile("" : "+v"(H[2 * hb]), "+v"(H[2 * hb + 1]));
      const u32x4 l0 = H[2 * hb], l1 = H[2 * hb + 1];
      H[2 * hb] = (u32x4){l0[0], l0[1], l1[0], l1[1]};
      H[2 * hb + 1] = (u32x4){l0[2], l0[3], l1[2], l1[3]};
      asm volatile("s_nop 1" : "+v"(H[2 * hb]), "+v"(H[2 * hb + 1]));
      SB0();
    }
    const u32x4 a = A[J % K4_D];
    mma16t<(J < 9)>(acc[2 * E.q], a, H[2 * E.hb]);
    if constexpr (J + K4_D < K4_N) A[J % K4_D] = rd(IC<J + K4_D>{});
    SB0();
    mma16t<(J < 9)>(acc[2 * E.q + 1], a, H[2 * E.hb + 1]);
    if constexpr (E.lk == L_X) LDH(E.la);
    if constexpr (E.lk == L_W) dma_piece<E.lb>(wsrc + E.la * UNIT, voff, wdst + (E.la & 3) * UNIT);
    if constexpr (E.lk == L_R) {
      if constexpr (E.la < 3)
        asm volatile("global_load_dwordx2 %0, %1, %2" : "=v"(xres[E.la]) : "v"(voff), "s"(E.la == 2 ? xf8 : xfA + E.la * 18432) : "memory");
      else if constexpr (E.la < 5)
        asm volatile("global_load_dwordx4 %0, %1, %2" : "=v"(gbv[E.la - 3]) : "v"(lane & 48), "s"(gbp + (E.la == 3 ? dA : d8) * 64) : "memory");
      else
        asm volatile("global_load_dword %0, %1, %2" : "=v"(stw[E.la - 5]) : "v"((lane & 15) * 4), "s"(stp + (E.la - 5) * 64) : "memory");
    }
    SB0();
  });
  WAIT_VM(0);
  asm volatile("" : "+v"(xres[0]), "+v"(xres[1]), "+v"(xres[2]), "+v"(gbv[0]), "+v"(gbv[1]), "+v"(stw[0]), "+v"(stw[1]));
  XBAR();
  GLBP unsigned* cnt = (GLBP unsigned*)(ws + OFF_CNT + tile * 128);
  if (tid == 0) __hip_atomic_fetch_add(cnt, 1u, __ATOMIC_RELAXED, __HIP_MEMORY_SCOPE_AGENT);
  MFMA_DRAIN();
  f32x4 fin[3] = {add4(acc[16], oth[0]), add4(acc[17], oth[1]), f8};
  float rs2[3];
#pragma unroll
  for (int i = 0; i < 2; ++i) { const float r = 1.f / hi16(stw[i]); rs2[i] = r * r; }
  rs2[2] = wk ? rs2[1] : rs2[0];
  if (wave == 0) {
    for (unsigned spins = 0; spins < (1u << 22); ++spins) {
      if ((unsigned)__builtin_amdgcn_readfirstlane(__hip_atomic_load(cnt, __ATOMIC_RELAXED, __HIP_MEMORY_SCOPE_AGENT)) >= 4u) break;
      __builtin_amdgcn_s_sleep(1);
    }
  }
  BARRIER();
  u32x4 pp[3];
  u32x2 p8[3];
#pragma unroll
  for (int j = 0; j < 3; ++j) {
    const int src = (kb + 1 + j) & 3;
    pp[j] = __builtin_amdgcn_raw_buffer_load_b128(prs, voff, ((kb * 4 + src) * 8 + wave) * 1024, 16);
    p8[j] = __builtin_amdgcn_raw_buffer_load_b64(prs, lane * 8, 131072 + ((kb * 4 + src) * 2 + wk) * 512, 16);
  }
#pragma unroll
  for (int i = 0; i < 3; ++i) {
    if (i == 2 && wm != kb) break;
    u32x2 o;
#pragma unroll
    for (int e = 0; e < 2; ++e) {
      const unsigned q0 = i < 2 ? pp[0][2 * i + e] : p8[0][e], q1 = i < 2 ? pp[1][2 * i + e] : p8[1][e], q2 = i < 2 ? pp[2][2 * i + e] : p8[2][e];
      float a = ((fin[i][2 * e] + lo16(q0)) + (lo16(q1) + lo16(q2))) * rs2[i];
      float b = ((fin[i][2 * e + 1] + hi16(q0)) + (hi16(q1) + hi16(q2))) * rs2[i];
      bfr2(a, b);
      const unsigned g0 = gbv[i >> 1][2 * e], g1 = gbv[i >> 1][2 * e + 1];
      a *= __uint_as_float(g0); b *= __uint_as_float(g1); bfr2(a, b);
      o[e] = pk2(lo16(xres[i][e]) + a, hi16(xres[i][e]) + b);
    }
    const int m = r0 + (i < 2 ? i : wk) * 16 + (lane & 15);
    *(u32x2*)(out + (size_t)m * D_ + (i < 2 ? dA : d8) * 16 + (lane >> 4) * 4) = o;
  }
}

extern "C" void kernel_launch(void* const* d_in, const int* in_sizes, int n_in, void* d_out, int out_size,
                              void* d_ws, size_t ws_size, hipStream_t stream) {
  const u16* A_q = (const u16*)d_in[0];
  const u16* A_k = (const u16*)d_in[1];
  const u16* A_v = (const u16*)d_in[2];
  const u16* A_sh = (const u16*)d_in[3];
  const u16* A_sk = (const u16*)d_in[4];
  const u16* A_D = (const u16*)d_in[5];
  const u16* Wg = (const u16*)d_in[7];
  const u16* Wu = (const u16*)d_in[8];
  const u16* Wd = (const u16*)d_in[9];
  const u16* cv = (const u16*)d_in[10];
  const u16* wm = (const u16*)d_in[11];
  char* ws = (char*)d_ws;
  (void)hipFuncSetAttribute((const void*)k1_conv, hipFuncAttributeMaxDynamicSharedMemorySize, K1_LDS);
  (void)hipFuncSetAttribute((const void*)k3_gemm1, hipFuncAttributeMaxDynamicSharedMemorySize, K3_LDS);
  (void)hipFuncSetAttribute((const void*)k4_gemm2, hipFuncAttributeMaxDynamicSharedMemorySize, K4_LDS);
  k0_prep<<<K0_GRID, 256, 0, stream>>>(A_q, A_k, A_v, A_sh, cv, wm, Wd, ws);
  k1_conv<<<K1_CONV + K1_W, 512, K1_LDS, stream>>>(A_sk, A_D, Wg, Wu, ws);
  k3_gemm1<<<256, 512, K3_LDS, stream>>>(Wg, Wu, ws);
  k4_gemm2<<<256, 512, K4_LDS, stream>>>(ws, (u16*)d_out);
}
```

```cpp
#include <hip/hip_runtime.h>
#include <hip/hip_bf16.h>
#include <stdint.h>

typedef unsigned short u16;
typedef __attribute__((ext_vector_type(2))) __bf16 bf16x2;
typedef __attribute__((ext_vector_type(8))) __bf16 bf16x8;
typedef __attribute__((ext_vector_type(2))) float f32x2;
typedef __attribute__((ext_vector_type(4))) float f32x4;
typedef __attribute__((ext_vector_type(16))) float f32x16;
typedef __attribute__((ext_vector_type(4))) unsigned u32x4;
typedef __attribute__((ext_vector_type(2))) unsigned u32x2;
typedef __attribute__((ext_vector_type(4))) short s16x4;
#define LDSP __attribute__((address_space(3)))
#define GLBP __attribute__((address_space(1)))

constexpr int S_ = 2048, D_ = 576, F_ = 4608;

constexpr size_t OFF_CNT = 0;
constexpr size_t OFF_MOD = 8192;
constexpr size_t OFF_ST1 = OFF_MOD + 16384;
constexpr size_t OFF_ST2 = OFF_ST1 + 8192;
constexpr size_t OFF_CC  = OFF_ST2 + 8192;
constexpr size_t OFF_UT  = OFF_CC + 32 * 6912;
constexpr size_t OFF_FLG = OFF_UT + (size_t)D_ * S_ * 2;
constexpr size_t OFF_XF  = OFF_FLG + 16384;
constexpr size_t OFF_XS  = OFF_XF + (size_t)D_ * S_ * 2;
constexpr size_t OFF_W2  = OFF_XS + (size_t)D_ * S_ * 2;
constexpr size_t OFF_H2  = OFF_W2 + (size_t)D_ * F_ * 2;
constexpr size_t OFF_PART = OFF_H2 + (size_t)S_ * F_ * 2;
constexpr int PART_TILE = 131072 + 16384;

__device__ __forceinline__ unsigned pk2(float a, float b) {
  f32x2 v = {a, b};
  return __builtin_bit_cast(unsigned, __builtin_convertvector(v, bf16x2));
}
__device__ __forceinline__ float lo16(unsigned u) { return __uint_as_float(u << 16); }
__device__ __forceinline__ float hi16(unsigned u) { return __uint_as_float(u & 0xffff0000u); }
__device__ __forceinline__ float bfr(float a) { return lo16(pk2(a, a)); }
__device__ __forceinline__ void bfr2(float& a, float& b) { unsigned u = pk2(a, b); a = lo16(u); b = hi16(u); }
__device__ __forceinline__ float bf2f(u16 x) { return __uint_as_float(((unsigned)x) << 16); }
__device__ __forceinline__ s16x4 tr_read(const char* p) {
  return __builtin_amdgcn_ds_read_tr16_b64_v4i16((LDSP s16x4*)p);
}
__device__ __forceinline__ u32x4 tr_read2(const char* p0, const char* p1) {
  s16x4 a = tr_read(p0), b = tr_read(p1);
  u32x2 x = __builtin_bit_cast(u32x2, a), y = __builtin_bit_cast(u32x2, b);
  u32x4 r = {x[0], x[1], y[0], y[1]};
  return r;
}

#define BARRIER() { asm volatile("s_waitcnt lgkmcnt(0)" ::: "memory"); __builtin_amdgcn_s_barrier(); asm volatile("" ::: "memory"); }
typedef LDSP const char* lptr;
__device__ __forceinline__ u32x4 tr_read2l(lptr p0, lptr p1) {
  s16x4 a = __builtin_amdgcn_ds_read_tr16_b64_v4i16((LDSP s16x4*)p0), b = __builtin_amdgcn_ds_read_tr16_b64_v4i16((LDSP s16x4*)p1);
  u32x2 x = __builtin_bit_cast(u32x2, a), y = __builtin_bit_cast(u32x2, b);
  u32x4 r = {x[0], x[1], y[0], y[1]};
  return r;
}

#define K0_AUX 16
constexpr int K0_MOD = 108, K0_KV = 288, K0_ST = 64;
constexpr int K0_GRID = K0_MOD + K0_KV + K0_ST;

__global__ __launch_bounds__(256) void k0_prep(const u16* __restrict__ xq, const u16* __restrict__ kk,
                                               const u16* __restrict__ vv, const u16* __restrict__ shift_k,
                                               const u16* __restrict__ cvec, const u16* __restrict__ w_mod,
                                               const u16* __restrict__ ssm_k, char* __restrict__ ws) {
  __shared__ __attribute__((aligned(16))) char sm[8192];
  const int tid = threadIdx.x, lane = tid & 63, wave = tid >> 6;
  int bid = blockIdx.x;
  if (bid < K0_MOD) {
    if (bid == 0 && tid < 64) *(unsigned*)(ws + OFF_CNT + tid * 128) = 0u;
    if (bid == 9) *(unsigned*)(ws + OFF_FLG + tid * 64) = 0u;
    if (bid >= 1 && bid <= 8) ((unsigned*)(ws + OFF_ST2))[(bid - 1) * 256 + tid] = 0u;
    const int c4 = tid & 3, rg = tid >> 2;
    u32x4 w[9];
    float sc[9];
#pragma unroll
    for (int i = 0; i < 9; ++i) {
      const int row = rg + 64 * i;
      w[i] = *(const u32x4*)(w_mod + (size_t)row * 3456 + bid * 32 + c4 * 8);
      sc[i] = bf2f(cvec[row]);
    }
    float acc[8];
#pragma unroll
    for (int e = 0; e < 8; ++e) acc[e] = 0.f;
#pragma unroll
    for (int i = 0; i < 9; ++i) {
      const float s = bfr(sc[i] / (1.f + __expf(-sc[i])));
#pragma unroll
      for (int e = 0; e < 4; ++e) {
        acc[2 * e] += s * lo16(w[i][e]);
        acc[2 * e + 1] += s * hi16(w[i][e]);
      }
    }
#pragma unroll
    for (int e = 0; e < 8; ++e) {
#pragma unroll
      for (int o = 4; o < 64; o <<= 1) acc[e] += __shfl_xor(acc[e], o);
    }
    float* red = (float*)sm;
    if (lane < 4) {
#pragma unroll
      for (int e = 0; e < 8; ++e) red[wave * 32 + lane * 8 + e] = acc[e];
    }
    __syncthreads();
    if (tid < 32) {
      const float r = (red[tid] + red[32 + tid]) + (red[64 + tid] + red[96 + tid]);
      ((float*)(ws + OFF_MOD))[bid * 32 + tid] = bfr(r);
    }
    return;
  }
  bid -= K0_MOD;
  if (bid < K0_KV) {
    const int cb = bid % 9, tb = bid / 9;
    const int c8 = tid & 7, rp = tid >> 3;
    const int r0 = tb * 64 + rp * 2;
    const int ch0 = cb * 64 + c8 * 8;
    u32x4 kr[5], vr[2], sw[4];
    unsigned warm = 0;
    if (tid < 128)
      asm volatile("global_load_dword %0, %1, %2" : "+v"(warm) : "v"(tid * 64), "s"((const char*)ssm_k + ((blockIdx.x & 7) * 36 + (bid >> 3)) * 8192) : "memory");
#pragma unroll
    for (int i = 0; i < 5; ++i) {
      const int r = r0 - 3 + i;
      const int rc = r < 0 ? 0 : r;
      kr[i] = *(const u32x4*)(kk + (size_t)rc * D_ + ch0);
      if (r < 0) kr[i] = (u32x4){0u, 0u, 0u, 0u};
    }
#pragma unroll
    for (int i = 0; i < 2; ++i) vr[i] = *(const u32x4*)(vv + (size_t)(r0 + i) * D_ + ch0);
#pragma unroll
    for (int i = 0; i < 4; ++i) sw[i] = *(const u32x4*)(shift_k + (size_t)ch0 * 4 + i * 8);
    char* tu = sm;
    const int q = rp >> 2, inner = rp & 3;
#pragma unroll
    for (int e = 0; e < 8; ++e) {
      const unsigned wa = sw[e >> 1][(e & 1) * 2], wb = sw[e >> 1][(e & 1) * 2 + 1];
      const float w0 = lo16(wa), w1 = hi16(wa), w2 = lo16(wb), w3 = hi16(wb);
      float kf[5];
#pragma unroll
      for (int i = 0; i < 5; ++i) kf[i] = (e & 1) ? hi16(kr[i][e >> 1]) : lo16(kr[i][e >> 1]);
      const float v0 = (e & 1) ? hi16(vr[0][e >> 1]) : lo16(vr[0][e >> 1]);
      const float v1 = (e & 1) ? hi16(vr[1][e >> 1]) : lo16(vr[1][e >> 1]);
      float a0 = kf[0] * w3, a1 = kf[1] * w3; bfr2(a0, a1);
      float t0 = kf[1] * w2, t1 = kf[2] * w2; bfr2(t0, t1);
      a0 += t0; a1 += t1; bfr2(a0, a1);
      t0 = kf[2] * w1; t1 = kf[3] * w1; bfr2(t0, t1);
      a0 += t0; a1 += t1; bfr2(a0, a1);
      t0 = kf[3] * w0; t1 = kf[4] * w0; bfr2(t0, t1);
      a0 += t0; a1 += t1; bfr2(a0, a1);
      const unsigned kvp = pk2(a0 * v0, a1 * v1);
      const int off = (c8 * 8 + e) * 128 + ((q ^ c8) * 16) + inner * 4;
      *(unsigned*)(tu + off) = kvp;
    }
    __syncthreads();
    asm volatile("s_waitcnt vmcnt(0)" : "+v"(warm));
    __amdgpu_buffer_rsrc_t urs = __builtin_amdgcn_make_buffer_rsrc((void*)(ws + OFF_UT), 0, D_ * S_ * 2, 0x00020000);
#pragma unroll
    for (int i = 0; i < 2; ++i) {
      const int p = tid + 256 * i, ch = p >> 3, qq = p & 7;
      const int off = ch * 128 + ((qq ^ (ch >> 3)) * 16);
      __builtin_amdgcn_raw_buffer_store_b128(*(const u32x4*)(tu + off), urs, ((cb * 64 + ch) * S_ + tb * 64 + qq * 8) * 2, 0, K0_AUX);
    }
    return;
  }
  bid -= K0_KV;
  {
    const int row = bid * 32 + wave * 8 + (lane >> 3), c = lane & 7;
    u32x4 xr[9];
#pragma unroll
    for (int i = 0; i < 9; ++i) xr[i] = *(const u32x4*)(xq + (size_t)row * D_ + (c + 8 * i) * 8);
    float s = 0.f;
#pragma unroll
    for (int i = 0; i < 9; ++i)
#pragma unroll
      for (int e = 0; e < 4; ++e) s += lo16(xr[i][e]) + hi16(xr[i][e]);
    s += __shfl_xor(s, 1); s += __shfl_xor(s, 2); s += __shfl_xor(s, 4);
    const float mean = s * (1.f / 576.f);
    float ss = 0.f;
#pragma unroll
    for (int i = 0; i < 9; ++i)
#pragma unroll
      for (int e = 0; e < 4; ++e) {
        const float a = lo16(xr[i][e]) - mean, b = hi16(xr[i][e]) - mean;
        ss += a * a + b * b;
      }
    ss += __shfl_xor(ss, 1); ss += __shfl_xor(ss, 2); ss += __shfl_xor(ss, 4);
    const float var = bfr(ss * (1.f / 576.f));
    const float sd = sqrtf(bfr(var + 1e-5f));
    if (c == 0) ((unsigned*)(ws + OFF_ST1))[row] = pk2(mean, sd);
  }
}

constexpr int K1_CONV = 144, K1_W = 108;
constexpr int KC_STRIDE = 4416;
constexpr int CH_BYTES = 4 * KC_STRIDE + 4672;
constexpr int K1_XCH = 4 * CH_BYTES;
constexpr int K1_X1 = K1_XCH + 32768;
constexpr int K1_LDS = K1_X1 + 32768;

#define XIT 1
#define K1_AUX 16
__global__ __launch_bounds__(512, 2) void k1_conv(const u16* __restrict__ xq, const u16* __restrict__ ssm_k, const u16* __restrict__ ssm_D,
                                                  const u16* __restrict__ Wg, const u16* __restrict__ Wu,
                                                  const u16* __restrict__ Wd, char* __restrict__ ws) {
  extern __shared__ __attribute__((aligned(16))) char sm[];
  const int tid = threadIdx.x, lane = tid & 63, wave = tid >> 6;
  const float* mod = (const float*)(ws + OFF_MOD);
  int bid = blockIdx.x;
  if (bid < K1_CONV) {
    const int cg = (bid & 7) * 18 + (bid >> 3);
    const int cl = wave >> 1, w2 = wave & 1;
    const int d = cg * 4 + cl;
    char* chb = sm + cl * CH_BYTES;
    char* ub = chb + 4 * KC_STRIDE;
    const int t128 = tid & 127;
    const char* krow = (const char*)ssm_k + (size_t)d * 4096;
    const char* urow = ws + OFF_UT + (size_t)d * 4096;
    u32x4 ka[2], ua[2];
    u32x2 kb[2];
#pragma unroll
    for (int i = 0; i < 2; ++i) {
      const int c = t128 + 128 * i;
      ka[i] = *(const u32x4*)(krow + c * 16);
      const int cn = c < 255 ? c + 1 : 255;
      kb[i] = *(const u32x2*)(krow + cn * 16);
      if (c == 255) kb[i] = (u32x2){0u, 0u};
      ua[i] = *(const u32x4*)(urow + c * 16);
    }
    const int li = lane & 15, g = lane >> 4;
    const float Dd = bf2f(ssm_D[d]);
    {
      const u32x4 z = {0u, 0u, 0u, 0u};
      if (t128 < 32) *(u32x4*)(ub + t128 * 16) = z;
      else if (t128 < 32 + 12) {
        const int i = t128 - 32;
        *(u32x4*)(chb + (i / 3) * KC_STRIDE + (i % 3) * 16) = z;
      } else if (t128 < 32 + 12 + 16) {
        const int i = t128 - 44;
        *(u32x4*)(chb + (i >> 2) * KC_STRIDE + 64 + 4096 + (i & 3) * 16) = z;
      } else if (t128 < 64) {
        *(u32x4*)(ub + 4608 + (t128 - 60) * 16) = z;
      }
    }
#pragma unroll
    for (int i = 0; i < 2; ++i) {
      const int c = t128 + 128 * i;
      const unsigned w0 = ka[i][0], w1 = ka[i][1], w2_ = ka[i][2], w3 = ka[i][3], w4 = kb[i][0], w5 = kb[i][1];
      const unsigned a0 = __builtin_amdgcn_alignbit(w1, w0, 16), a1 = __builtin_amdgcn_alignbit(w2_, w1, 16),
                     a2 = __builtin_amdgcn_alignbit(w3, w2_, 16), a3 = __builtin_amdgcn_alignbit(w4, w3, 16),
                     a4 = __builtin_amdgcn_alignbit(w5, w4, 16);
      char* p = chb + 64 + c * 16;
      *(u32x4*)(p) = ka[i];
      *(u32x4*)(p + KC_STRIDE) = (u32x4){a0, a1, a2, a3};
      *(u32x4*)(p + 2 * KC_STRIDE) = (u32x4){w1, w2_, w3, w4};
      *(u32x4*)(p + 3 * KC_STRIDE) = (u32x4){a1, a2, a3, a4};
      *(u32x4*)(ub + 512 + c * 16) = ua[i];
      if (c == 0) {
        *(u32x4*)(p - 16) = (u32x4){0u, 0u, 0u, 0u};
        *(u32x4*)(p - 16 + KC_STRIDE) = (u32x4){0u, 0u, 0u, w0 << 16};
        *(u32x4*)(p - 16 + 2 * KC_STRIDE) = (u32x4){0u, 0u, 0u, w0};
        *(u32x4*)(p - 16 + 3 * KC_STRIDE) = (u32x4){0u, 0u, w0 << 16, a0};
      }
    }
    u32x2 xv[4];
    unsigned st[4];
#pragma unroll
    for (int i = 0; i < 4; ++i) {
      const int s = (wave * 8 + (i >> 1) * 4 + g) * 32 + (i & 1) * 16 + li;
      st[i] = ((const unsigned*)(ws + OFF_ST1))[s];
    }
    const f32x4 sa = *(const f32x4*)(mod + cg * 4), ca = *(const f32x4*)(mod + 576 + cg * 4);
    const f32x4 ga = *(const f32x4*)(mod + 1152 + cg * 4), cb = *(const f32x4*)(mod + 2304 + cg * 4);
    BARRIER();
    const int q = li >> 2, p4 = li & 3;
    const char* abase = chb + ((4 - q) & 3) * KC_STRIDE + 2 * (32 - 8 * g - (q ? 4 : 0) + 4 * p4) - 8;
    const char* bbase = ub + 2 * (256 + 16 * li + 8 * g);
    f32x4 acc[8];
#pragma unroll
    for (int b = 0; b < 8; ++b) acc[b] = (f32x4){0.f, 0.f, 0.f, 0.f};
    u32x2 hp[4], tp[4];
    auto hfun = [&](int i) {
      const float mu = lo16(st[i]), rs = __builtin_amdgcn_rcpf(hi16(st[i]));
#pragma unroll
      for (int e = 0; e < 2; ++e) {
        float h0 = lo16(xv[i][e]) - mu, h1 = hi16(xv[i][e]) - mu; bfr2(h0, h1);
        h0 *= rs; h1 *= rs; bfr2(h0, h1);
        h0 *= bfr(1.f + ca[2 * e]); h1 *= bfr(1.f + ca[2 * e + 1]); bfr2(h0, h1);
        hp[i][e] = pk2(h0 + sa[2 * e], h1 + sa[2 * e + 1]);
      }
    };
#pragma unroll
    for (int it = 0; it < 4; ++it) {
      const int r = 2 * it + 1 + w2;
      const char* ap = abase + 64 * r;
      const char* bp = bbase - 64 * r;
      bf16x8 A[8], B[8];
#pragma unroll
      for (int i = 0; i < 8; ++i) {
        A[i] = __builtin_bit_cast(bf16x8, tr_read2(ap + 512 * i + 8, ap + 512 * i));
        B[i] = *(const bf16x8*)(bp + 512 * i);
      }
      if (it < 2) {
#pragma unroll
        for (int i = 2 * it; i < 2 * it + 2; ++i) {
          const u32x2 uu = *(const u32x2*)(ub + 512 + (256 * (4 * w2 + i) + 16 * li + 4 * g) * 2);
          tp[i] = (u32x2){pk2(lo16(uu[0]) * Dd, hi16(uu[0]) * Dd), pk2(lo16(uu[1]) * Dd, hi16(uu[1]) * Dd)};
        }
      }
      if (it == 3) { hfun(0); hfun(1); }
#pragma unroll
      for (int b = 0; b < 8; ++b)
#pragma unroll
        for (int qq = 0; qq <= b; ++qq)
        {
          acc[b] = __builtin_amdgcn_mfma_f32_16x16x32_bf16(A[qq], B[b - qq], acc[b], 0, 0, 0);
          if (qq == 0 && ((it < XIT && (b == 2 || b == 6)) || (it < 4 - XIT && it >= XIT && b == 2))) {
            const int i = it < XIT ? 2 * it + (b == 6) : it + XIT;
            __builtin_amdgcn_sched_barrier(0);
            xv[i] = *(const u32x2*)(xq + (size_t)((wave * 8 + (i >> 1) * 4 + g) * 32 + (i & 1) * 16 + li) * D_ + cg * 4);
            __builtin_amdgcn_sched_barrier(0);
          }
        }
    }
    char* xch = sm + K1_XCH;
    f32x4 fin[4];
    if (w2 == 0) {
#pragma unroll
      for (int i = 0; i < 4; ++i) { *(f32x4*)(xch + wave * 4096 + i * 1024 + lane * 16) = acc[4 + i]; fin[i] = acc[i]; }
    } else {
#pragma unroll
      for (int i = 0; i < 4; ++i) { *(f32x4*)(xch + wave * 4096 + i * 1024 + lane * 16) = acc[i]; fin[i] = acc[4 + i]; }
    }
    {
      const bf16x8 A0 = __builtin_bit_cast(bf16x8, tr_read2(abase + 8, abase));
#pragma unroll
      for (int i = 0; i < 4; ++i) {
        const bf16x8 Bd = *(const bf16x8*)(bbase + 512 * (4 * w2 + i));
        fin[i] = __builtin_amdgcn_mfma_f32_16x16x32_bf16(A0, Bd, fin[i], 0, 0, 0);
      }
    }
    hfun(2); hfun(3);
    __syncthreads();
    char* ys = sm + K1_X1;
#pragma unroll
    for (int i = 0; i < 4; ++i) {
      const f32x4 o = *(const f32x4*)(xch + (wave ^ 1) * 4096 + i * 1024 + lane * 16);
      u32x2 y;
#pragma unroll
      for (int e = 0; e < 2; ++e) {
        float y0 = fin[i][2 * e] + o[2 * e], y1 = fin[i][2 * e + 1] + o[2 * e + 1]; bfr2(y0, y1);
        y[e] = pk2(y0 + lo16(tp[i][e]), y1 + hi16(tp[i][e]));
      }
      *(u32x2*)(ys + cl * 4128 + (256 * (4 * w2 + i) + 16 * li + 4 * g) * 2) = y;
    }
    __syncthreads();
    __amdgpu_buffer_rsrc_t xrs = __builtin_amdgcn_make_buffer_rsrc((void*)(ws + OFF_XF), 0, 2 * D_ * S_ * 2, 0x00020000);
#pragma unroll
    for (int j = 0; j < 2; ++j) {
      const int mt = wave * 8 + j * 4 + g;
      const char* yp = ys + q * 4128 + mt * 64 + p4 * 8;
      const u32x4 y = tr_read2(yp, yp + 32);
      u32x4 o1, o2;
#pragma unroll
      for (int e = 0; e < 4; ++e) {
        const unsigned hh = hp[2 * j + (e >> 1)][e & 1], xx = xv[2 * j + (e >> 1)][e & 1];
        const int c = (e & 1) * 2;
        float y0 = lo16(y[e]) * lo16(hh), y1 = hi16(y[e]) * hi16(hh); bfr2(y0, y1);
        y0 *= ga[c]; y1 *= ga[c + 1]; bfr2(y0, y1);
        const unsigned r = pk2(lo16(xx) + y0, hi16(xx) + y1);
        o1[e] = r;
        o2[e] = pk2(lo16(r) * bfr(1.f + cb[c]), hi16(r) * bfr(1.f + cb[c + 1]));
      }
      const int off = mt * 36864 + cg * 256 + li * 16;
      __builtin_amdgcn_raw_buffer_store_b128(o2, xrs, off, D_ * S_ * 2, K1_AUX);
      __builtin_amdgcn_raw_buffer_store_b128(o1, xrs, off, 0, K1_AUX);
    }
    return;
  }
  bid -= K1_CONV;
  {
    const int cbk = bid % 36, kt = bid / 36;
    const int mat = cbk / 18, cblk = cbk % 18;
    const u16* W = mat ? Wu : Wg;
    const int p = tid & 31, rg = tid >> 5;
    u32x4 w[12];
    float cs[12], sb[12];
#pragma unroll
    for (int i = 0; i < 12; ++i) {
      const int k = kt * 192 + rg + 16 * i;
      w[i] = *(const u32x4*)(W + (size_t)k * F_ + cblk * 256 + p * 8);
    }
    __builtin_amdgcn_sched_barrier(0);
#pragma unroll
    for (int i = 0; i < 12; ++i) {
      const int k = kt * 192 + rg + 16 * i;
      cs[i] = mod[4 * 576 + k];
      sb[i] = mod[3 * 576 + k];
    }
    const int pc = min(__builtin_amdgcn_readfirstlane(wave) * K1_W + bid, 575), T = pc >> 2, nt = pc & 3;
    u32x4 wd[9];
    if (wave * K1_W + bid < 576) {
#pragma unroll
      for (int i = 0; i < 9; ++i) {
        const int c = i * 64 + lane;
        wd[i] = *(const u32x4*)((const char*)Wd + (size_t)T * 36864 + nt * 288 + (c / 18) * 1152 + (c % 18) * 16);
      }
    }
    __builtin_amdgcn_sched_barrier(0);
    float a1[8], a2[8];
#pragma unroll
    for (int e = 0; e < 8; ++e) { a1[e] = 0.f; a2[e] = 0.f; }
#pragma unroll
    for (int i = 0; i < 12; ++i) {
      const float c1 = bfr(1.f + cs[i]);
#pragma unroll
      for (int e = 0; e < 4; ++e) {
        const float l = lo16(w[i][e]), h = hi16(w[i][e]);
        a1[2 * e] += c1 * l; a1[2 * e + 1] += c1 * h;
        a2[2 * e] += sb[i] * l; a2[2 * e + 1] += sb[i] * h;
      }
    }
    float* red = (float*)(sm + 110592);
#pragma unroll
    for (int e = 0; e < 8; ++e) { a1[e] += __shfl_xor(a1[e], 32); a2[e] += __shfl_xor(a2[e], 32); }
    if (lane < 32) {
      *(f32x4*)(red + wave * 512 + p * 8) = (f32x4){a1[0], a1[1], a1[2], a1[3]};
      *(f32x4*)(red + wave * 512 + p * 8 + 4) = (f32x4){a1[4], a1[5], a1[6], a1[7]};
    } else {
      *(f32x4*)(red + wave * 512 + 256 + p * 8) = (f32x4){a2[0], a2[1], a2[2], a2[3]};
      *(f32x4*)(red + wave * 512 + 256 + p * 8 + 4) = (f32x4){a2[4], a2[5], a2[6], a2[7]};
    }
    BARRIER();
    {
      float r = 0.f;
#pragma unroll
      for (int i = 0; i < 8; ++i) r += red[i * 512 + tid];
      const int which = tid >> 8, col = tid & 255;
      const int ft = cblk * 16 + (col >> 4);
      ((float*)(ws + OFF_CC))[(ft / 9) * 1728 + kt * 576 + which * 288 + (ft % 9) * 32 + mat * 16 + (col & 15)] = r;
    }
    if (wave * K1_W + bid < 576) {
      char* im = sm + wave * 9216;
#pragma unroll
      for (int i = 0; i < 9; ++i) *(u32x4*)(im + i * 1024 + lane * 16) = wd[i];
      const char* base = im + ((lane >> 4) * 4 + ((lane & 15) >> 2)) * 288 + (lane & 3) * 8;
      const int kb = T / 36, Tl = T % 36;
      __amdgpu_buffer_rsrc_t wrs = __builtin_amdgcn_make_buffer_rsrc((void*)(ws + OFF_W2 + (size_t)(kb * 4 + nt) * 331776), 0, 331776, 0x00020000);
      asm volatile("s_waitcnt lgkmcnt(0)" ::: "memory");
#pragma unroll
      for (int i = 0; i < 9; ++i) {
        const u32x4 r = tr_read2(base + i * 32, base + i * 32 + 16 * 288);
        const int o = ((i >> 1) - kb) & 3;
        const int pos = i == 8 ? 6 : (o ? 2 * (o - 1) + (i & 1) : 7 + (i & 1));
        __builtin_amdgcn_raw_buffer_store_b128(r, wrs, lane * 16, (Tl < 24 ? Tl * 9 + pos : 216 + pos * 12 + Tl - 24) * 1024, K1_AUX);
      }
    }
  }
}

constexpr int UNIT = 36864;

__device__ __forceinline__ void dma_unit(const char* gbase  , unsigned voff, unsigned lds_dst  ) {
  unsigned keep;
  asm volatile(
      "s_mov_b32 %0, m0\n\t"
      "s_mov_b32 m0, %3\n\t"
      "s_mov_b32 exec_hi, 0\n\t"
      "global_load_lds_dwordx4 %1, %2\n\t"
      "s_mov_b32 exec_hi, -1\n\t"
      "global_load_lds_dwordx4 %1, %2 offset:512\n\t"
      "global_load_lds_dwordx4 %1, %2 offset:1536\n\t"
      "global_load_lds_dwordx4 %1, %2 offset:2560\n\t"
      "global_load_lds_dwordx4 %1, %2 offset:3584\n\t"
      "s_mov_b32 m0, %0"
      : "=&s"(keep)
      : "v"(voff), "s"(gbase), "s"(lds_dst)
      : "memory");
}
template <int OFF, bool SC1 = false>
__device__ __forceinline__ void ld_frag(u32x4& dst, const char* gbase  , unsigned voff) {
  if constexpr (SC1) asm volatile("global_load_dwordx4 %0, %1, %2 offset:%3 sc1" : "=v"(dst) : "v"(voff), "s"(gbase), "i"(OFF) : "memory");
  else asm volatile("global_load_dwordx4 %0, %1, %2 offset:%3" : "=v"(dst) : "v"(voff), "s"(gbase), "i"(OFF) : "memory");
}
#define PSL 1
#define H_SC1 1
#define TS_STEP(i)
#define H2_AUX 16
#define WAIT_VM(n) asm volatile("s_waitcnt vmcnt(" #n ")" ::: "memory")
#define PIN4(a, b, c, d) asm volatile("" : "+v"(a), "+v"(b), "+v"(c), "+v"(d))

constexpr int K3_UP = 18432;
constexpr int K3_CCRAW = 4 * UNIT;
constexpr int K3_CC = K3_CCRAW + 6912;
constexpr int K3_LDS = K3_CC + 4608;
constexpr int K3_D = 6;

__device__ __forceinline__ void dma_rows(const char* gbase  , const unsigned (&vo)[5], unsigned lds_dst  ) {
  asm volatile(
      "s_mov_b32 m0, %6\n\t"
      "s_mov_b32 exec_hi, 0\n\t"
      "global_load_lds_dwordx4 %0, %5\n\t"
      "s_mov_b32 exec_hi, -1\n\t"
      "global_load_lds_dwordx4 %1, %5 offset:512\n\t"
      "global_load_lds_dwordx4 %2, %5 offset:1536\n\t"
      "global_load_lds_dwordx4 %3, %5 offset:2560\n\t"
      "global_load_lds_dwordx4 %4, %5 offset:3584"
      :
      : "v"(vo[0]), "v"(vo[1]), "v"(vo[2]), "v"(vo[3]), "v"(vo[4]), "s"(gbase), "s"(lds_dst)
      : "memory");
}
#define SB0() __builtin_amdgcn_sched_barrier(0)
#define XBAR() { SB0(); asm volatile("s_barrier" ::: "memory"); SB0(); }

template <int V> struct IC { static constexpr int value = V; };
template <int I0, int I1, class F>
__device__ __forceinline__ void static_for(F&& f) {
  if constexpr (I0 < I1) { f(IC<I0>{}); static_for<I0 + 1, I1>(f); }
}
template <int J>
__device__ __forceinline__ void dma_piece(const char* gbase  , unsigned vo, unsigned lds_dst  ) {
  if constexpr (J == 0)
    asm volatile("s_mov_b32 m0, %2\n\ts_mov_b32 exec_hi, 0\n\tglobal_load_lds_dwordx4 %0, %1\n\ts_mov_b32 exec_hi, -1"
                 :: "v"(vo), "s"(gbase), "s"(lds_dst) : "memory");
  else
    asm volatile("s_mov_b32 m0, %2\n\tglobal_load_lds_dwordx4 %0, %1 offset:%3"
                 :: "v"(vo), "s"(gbase), "s"(lds_dst), "i"(1024 * J - 512) : "memory");
}
__device__ __forceinline__ f32x4 mma16(u32x4 a, u32x4 b, f32x4 c) {
  return __builtin_amdgcn_mfma_f32_16x16x32_bf16(__builtin_bit_cast(bf16x8, a), __builtin_bit_cast(bf16x8, b), c, 0, 0, 0);
}
template <bool Z>
__device__ __forceinline__ void mma16t(f32x4& c, u32x4 a, u32x4 b) {
  if constexpr (Z) asm volatile("v_mfma_f32_16x16x32_bf16 %0, %1, %2, 0" : "=v"(c) : "v"(a), "v"(b));
  else asm volatile("v_mfma_f32_16x16x32_bf16 %0, %1, %2, %0" : "+v"(c) : "v"(a), "v"(b));
}
#define MFMA_DRAIN() { asm volatile("s_nop 7\n\ts_nop 7" ::: "memory"); SB0(); }
__device__ __forceinline__ float relu(float a) { return __int_as_float(max(__float_as_int(a), 0)); }
__device__ __forceinline__ u32x2 reglu(f32x4 g, f32x4 u) {
  float p[4];
#pragma unroll
  for (int i = 0; i < 4; ++i) asm("v_mul_f32 %0, %1, %2" : "=v"(p[i]) : "v"(relu(g[i])), "v"(u[i]));
  return (u32x2){pk2(p[0], p[1]), pk2(p[2], p[3])};
}

enum { L_NONE = 0, L_X, L_W, L_CC, L_POLL, L_R };
struct K3E {
  int pos, u, k32, tm, off;
  int epi;
  int lk, la, lb;
};
constexpr int K3_N = 342;
struct K3T { K3E e[K3_N]; };
constexpr K3T k3_table() {
  K3T t{};
  int n = 0;
  for (int i = 0; i < 324; ++i) {
    const int u = i / 36, r = i % 36;
    K3E e{};
    e.pos = i; e.u = u; e.epi = -1;
    if (u < 6) {
      e.k32 = 2 * u + r / 18; e.tm = r % 18;
      e.off = (e.tm & 1) * K3_UP + (r / 18) * 9216 + (e.tm >> 1) * 32;
    } else {
      e.k32 = 12 + (r % 12) / 2; e.tm = ((u - 6) * 3 + r / 12) * 2 + (r & 1);
      e.off = (r & 1) * K3_UP + ((r % 12) / 2) * 3072 + (r / 12) * 32;
      if (i > 228 && r % 12 == 2) e.epi = (e.tm >> 1) - 1;
    }
    if (u == 0) {
      if (r < 5) { e.lk = L_W; e.la = 1; e.lb = r; }
      else if (r == 5) e.lk = L_CC;
      else if (r >= 8 && r <= 20 && r % 3 == 2) { e.lk = L_W; e.la = 2; e.lb = (r - 8) / 3; }
      else if (r >= 23 && r <= 32 && r % 3 == 2) { e.lk = L_X; e.la = 8 + (r - 23) / 3; }
    } else if (u == 5) {
      if (r < 4) { e.lk = L_X; e.la = 32 + r; }
      else if (r >= 6 && r <= 18 && r % 3 == 0) { e.lk = L_W; e.la = 7; e.lb = (r - 6) / 3; }
      else if (r == 21) e.lk = L_POLL;
    } else if (u <= 6) {
      if (r <= 12 && r % 3 == 0) { e.lk = L_W; e.la = u + 2; e.lb = r / 3; }
      else if (u < 4 && r >= 15 && r <= 24 && r % 3 == 0) { e.lk = L_X; e.la = 8 + 4 * u + (r - 15) / 3; }
      else if (u == 4 && r >= 15 && r <= 29 && (r & 1)) { e.lk = L_X; e.la = 24 + (r - 15) / 2; }
    } else if (r <= 16 && r % 4 == 0) { e.lk = L_R; e.la = u - 7; e.lb = r / 4; }
    t.e[n++] = e;
    if (u >= 6 && r % 12 == 11) {
      K3E c{};
      c.pos = -1; c.epi = -1;
      c.tm = e.tm - 1; c.off = c.tm * 256; t.e[n++] = c;
      c.tm = e.tm; c.off = c.tm * 256; t.e[n++] = c;
    }
  }
  return t;
}
constexpr K3T K3TAB = k3_table();
constexpr int k3_younger(int J, int lk, int la, int lb) {
  int n = 0;
  for (int j = 0; j < J; ++j) {
    const K3E& e = K3TAB.e[j];
    if (e.epi >= 0) ++n;
    if (e.lk) n += e.lk == L_POLL ? 2 : 1;
    if (e.lk == lk && e.la == la && e.lb == lb) n = 0;
  }
  return n;
}
template <int N> __device__ __forceinline__ void wait_vm() { asm volatile("s_waitcnt vmcnt(%0)" :: "n"(N) : "memory"); }

__device__ __forceinline__ void k3_gemm1(const u16* __restrict__ Wg, const u16* __restrict__ Wu, char* __restrict__ ws, char* sm) {
  const int tid = threadIdx.x, lane = tid & 63;
  const int wave = __builtin_amdgcn_readfirstlane(tid >> 6);
  const int bid = blockIdx.x;
  const int xcd = bid & 7, idx = bid >> 3;
  const int m256 = (xcd >> 2) * 4 + (idx >> 3), sl = (xcd & 3) * 8 + (idx & 7);
  const int mt = m256 * 8 + wave;
  const unsigned voff = lane * 16;
  const unsigned lds0 = (unsigned)(uintptr_t)(LDSP char*)sm;
  const char* wA = (const char*)((wave & 4) ? Wu : Wg) + (size_t)(wave & 3) * 16 * 9216 + sl * 288;
  const char* wB = (const char*)((wave & 4) ? Wu : Wg) + (size_t)(384 + (wave & 3) * 48) * 9216 + sl * 288;
  const unsigned wdst = lds0 + wave * 4608;
  unsigned vo[5];
#pragma unroll
  for (int j = 0; j < 5; ++j) {
    const unsigned off = j ? 1024 * j - 512 : 0, c = off / 16 + lane;
    vo[j] = (c / 18) * 9216 + (c % 18) * 16 - off;
  }
  const char* xsrc = ws + OFF_XS + (size_t)mt * 36864;
  u32x4 X[36];
#define LDX(i) ld_frag<((i) & 3) * 1024>(X[i], xsrc + ((i) >> 2) * 4096, voff)
#define SWP(p, i, q, j) { unsigned a_ = p[i], c_ = q[j]; asm volatile("v_swap_b32 %0, %1" : "+v"(a_), "+v"(c_)); p[i] = a_; q[j] = c_; \
  asm volatile("" : "+v"(p), "+v"(q)); }
#define FIX2(i) { SWP(X[i], 2, X[(i) + 1], 0) SWP(X[i], 3, X[(i) + 1], 1) }
#define FIX4(i) { PIN4(X[i], X[(i) + 1], X[(i) + 2], X[(i) + 3]); FIX2(i) FIX2((i) + 2) \
  asm volatile("s_nop 1" : "+v"(X[i]), "+v"(X[(i) + 1]), "+v"(X[(i) + 2]), "+v"(X[(i) + 3])); }
#define LDX4(i) { LDX(i); LDX((i) + 1); LDX((i) + 2); LDX((i) + 3); }
  dma_rows(wA, vo, wdst + 3 * UNIT);
  LDX4(0)
  XBAR();
  LDX4(4)
  u32x4 sx[9];
  if (wave == 7) {
    const char* sb = ws + OFF_XF + (size_t)(m256 * 8 + (sl >> 2)) * 36864 + (sl & 3) * 64;
    const unsigned so = (lane >> 2) * 256 + (lane & 3) * 16;
#pragma unroll
    for (int i = 0; i < 9; ++i) ld_frag<0>(sx[i], sb + i * 4096, so);
    WAIT_VM(13);
  } else {
    WAIT_VM(4);
  }
  const char* csrc = ws + OFF_CC + (size_t)sl * 6912 + wave * 864;
  const char* w2src = ws + OFF_W2 + (size_t)((xcd & 3) * 4 + (idx & 3)) * 324 * 1024 + wave * 4608;
  const unsigned cdst = lds0 + K3_CCRAW + wave * 864;
  f32x4 acc[36];
  __amdgpu_buffer_rsrc_t hrs = __builtin_amdgcn_make_buffer_rsrc((void*)(ws + OFF_H2 + (size_t)mt * 294912), 0, 294912, 0x00020000);
  const int rw = (lane >> 4) * 4 + ((lane & 15) >> 2);
  lptr bu = (lptr)sm;
  lptr cp = (lptr)sm + K3_CC + (lane & 15) * 16;
  u32x4 A[K3_D];
  unsigned stw[2];
  u32x4 bc[2];
  const char* stp = ws + OFF_ST2 + (size_t)mt * 128;
  auto rd = [&](auto jc, auto hc) -> u32x2 {
    constexpr int H = decltype(hc)::value;
    constexpr K3E E = K3TAB.e[decltype(jc)::value];
    if constexpr (E.pos >= 0) {
      constexpr int RS = E.u < 6 ? 288 : 96;
      if constexpr (E.pos % 36 == 0 && H == 0) { bu = (lptr)sm + ((E.u + 3) & 3) * UNIT + rw * RS + (lane & 3) * 8; asm volatile("" : "+v"(bu)); }
      return __builtin_bit_cast(u32x2, __builtin_amdgcn_ds_read_tr16_b64_v4i16((LDSP s16x4*)(bu + E.off + H * 16 * RS)));
    } else {
      return *(LDSP const u32x2*)(cp + E.off + H * 8);
    }
  };
  auto epi = [&](auto tc) {
    constexpr int T = decltype(tc)::value, c = T * 4;
    const u32x2 h0 = reglu(acc[c], acc[c + 2]), h1 = reglu(acc[c + 1], acc[c + 3]);
    __builtin_amdgcn_raw_buffer_store_b128((u32x4){h0[0], h0[1], h1[0], h1[1]}, hrs, voff, (sl * 9 + T) * 1024, H2_AUX);
    SB0();
  };
#define POLL() asm volatile("global_load_dword %0, %2, %3 sc1\n\tglobal_load_dword %1, %2, %3 offset:64 sc1" \
                            : "=&v"(stw[0]), "=&v"(stw[1]) : "v"((lane & 15) * 4), "s"(stp) : "memory")
  FIX4(0)
  XBAR();
  static_for<0, K3_D>([&](auto jc) {
    const u32x2 n0 = rd(jc, IC<0>{}), n1 = rd(jc, IC<1>{});
    A[decltype(jc)::value] = (u32x4){n0[0], n0[1], n1[0], n1[1]};
  });
  if (wave == 7) {
    WAIT_VM(0);
    PIN4(sx[0], sx[1], sx[2], sx[3]); PIN4(sx[4], sx[5], sx[6], sx[7]); asm volatile("" : "+v"(sx[8]));
    const bf16x2 ones = __builtin_bit_cast(bf16x2, 0x3f803f80u);
    float s1[2] = {0.f, 0.f}, s2[2] = {0.f, 0.f};
#pragma unroll
    for (int i = 0; i < 9; ++i)
#pragma unroll
      for (int e = 0; e < 4; ++e) {
        const unsigned xu = sx[i][e];
        const bf16x2 xx = __builtin_bit_cast(bf16x2, xu);
        s1[e >> 1] = __builtin_amdgcn_fdot2_f32_bf16(xx, ones, s1[e >> 1], false);
        s2[e >> 1] = __builtin_amdgcn_fdot2_f32_bf16(xx, xx, s2[e >> 1], false);
      }
    const bool up = lane & 4;
    float a = (up ? s1[1] : s1[0]) + __shfl_xor(up ? s1[0] : s1[1], 4), b = (up ? s2[1] : s2[0]) + __shfl_xor(up ? s2[0] : s2[1], 4);
#pragma unroll
    for (int o = 8; o < 64; o <<= 1) { a += __shfl_xor(a, o); b += __shfl_xor(b, o); }
    const float mean = a * (1.f / 576.f);
    const float var = fmaxf(b * (1.f / 576.f) - mean * mean, 0.f);
    const unsigned w = pk2(mean, sqrtf(bfr(bfr(var) + 1e-5f)));
    if (lane < 8)
      __hip_atomic_store((GLBP unsigned*)(ws + OFF_ST2) + m256 * 256 + (sl >> 2) * 32 + (sl & 3) * 4 + (lane & 3) + (lane & 4) * 4, w, __ATOMIC_RELAXED, __HIP_MEMORY_SCOPE_AGENT);
  }
  SB0();
  static_for<0, K3_N>([&](auto jc) {
    constexpr int J = decltype(jc)::value;
    constexpr K3E E = K3TAB.e[J];
    constexpr int I = E.pos;
    if constexpr (I >= 0 && I % 36 == 30 && I < 300) {
      wait_vm<k3_younger(J, I == 30 ? L_CC : L_W, I == 30 ? 0 : E.u + 1, I == 30 ? 0 : 4)>();
      XBAR();
      TS_STEP(E.u + 2)
    }
    if constexpr (I == 32) {
      if (tid < 288) {
        const float* rp = (const float*)(sm + K3_CCRAW) + tid;
        const float c1 = rp[0] + rp[576] + rp[1152], c2 = rp[288] + rp[864] + rp[1440];
        const float h1 = bfr(c1), r1 = c1 - h1, m1 = bfr(r1), h2 = bfr(c2), r2 = c2 - h2, m2 = bfr(r2);
        *(u32x4*)(sm + K3_CC + tid * 16) = (u32x4){pk2(h1, m1), pk2(r1 - m1, h2), pk2(m2, r2 - m2), 0u};
      }
      SB0();
    }
    if constexpr (I > 0 && (I <= 216 ? I % 36 == 0 : (I == 220 || I == 224))) {
      constexpr int x0 = E.k32 * 2;
      wait_vm<k3_younger(J, L_X, x0 + 3, 0)>();
      FIX4(x0)
      SB0();
    }
    if constexpr (I == 130) {
#pragma unroll
      for (int j = 0; j < 5; ++j) {
        const unsigned off = j ? 1024 * j - 512 : 0, c = off / 16 + lane;
        vo[j] = (c / 6) * 9216 + (c % 6) * 16 - off;
      }
      SB0();
    }
    if constexpr (I == 226) {
      wait_vm<k3_younger(J, L_POLL, 0, 0)>();
      asm volatile("" : "+v"(stw[0]), "+v"(stw[1]));
      for (unsigned spins = 0; __builtin_amdgcn_ballot_w64(stw[0] == 0u || stw[1] == 0u) != 0ull && spins < (1u << 20); ++spins) {
        __builtin_amdgcn_s_sleep(2);
        POLL(); WAIT_VM(0); asm volatile("" : "+v"(stw[0]), "+v"(stw[1]));
      }
#pragma unroll
      for (int m = 0; m < 2; ++m) {
        const unsigned w = lane < 16 ? (stw[m] ^ 0x8000u) : 0u;
        const unsigned nm = w & 0xffffu, sd = w >> 16;
        bc[m] = (u32x4){nm | (nm << 16), w, sd | (sd << 16), 0u};
      }
      asm volatile("" : "+v"(bc[0]), "+v"(bc[1]));
      SB0();
    }
    if constexpr (E.epi >= 0) epi(IC<E.epi>{});
    const u32x4 a = A[J % K3_D];
    constexpr bool Z = I >= 0 && I < 18;
    u32x2 n0, n1;
    if constexpr (I >= 0) mma16t<Z>(acc[2 * E.tm], a, X[E.k32 * 2]); else mma16t<false>(acc[2 * E.tm], a, bc[0]);
    if constexpr (J + K3_D < K3_N) n0 = rd(IC<J + K3_D>{}, IC<0>{});
    SB0();
    if constexpr (I >= 0) mma16t<Z>(acc[2 * E.tm + 1], a, X[E.k32 * 2 + 1]); else mma16t<false>(acc[2 * E.tm + 1], a, bc[1]);
    if constexpr (J + K3_D < K3_N) { n1 = rd(IC<J + K3_D>{}, IC<1>{}); A[J % K3_D] = (u32x4){n0[0], n0[1], n1[0], n1[1]}; }
    if constexpr (E.lk == L_X) LDX(E.la);
    if constexpr (E.lk == L_W)
      dma_piece<E.lb>(E.la < 6 ? wA + (size_t)E.la * 64 * 9216 : wB + (E.la - 6) * 96, vo[E.lb], wdst + ((E.la + 3) & 3) * UNIT);
    if constexpr (E.lk == L_R) dma_piece<E.lb>(w2src + E.la * UNIT, voff, wdst + E.la * UNIT);
    if constexpr (E.lk == L_CC)
      asm volatile("s_mov_b32 m0, %2\n\ts_mov_b32 exec_hi, 0x3fffff\n\tglobal_load_lds_dwordx4 %0, %1\n\ts_mov_b32 exec_hi, -1"
                   :: "v"(voff), "s"(csrc), "s"(cdst) : "memory");
    if constexpr (E.lk == L_POLL) POLL();
    SB0();
  });
  MFMA_DRAIN();
  epi(IC<8>{});
}

constexpr int K4_LDS = 4 * UNIT + 16384;
constexpr int K4_N = 162, K4_D = 6;
struct K4E { int u, q, hb, b, off, lk, la, lb; };
struct K4T { K4E e[K4_N]; };
constexpr K4T k4_table() {
  K4T t{};
  for (int i = 0; i < K4_N; ++i) {
    const int u = i / 18, r = i % 18;
    K4E e{};
    e.u = u;
    e.q = u < 6 ? r % 9 : (u - 6) * 3 + r / 6;
    e.hb = u < 6 ? 2 * u + r / 9 : 12 + r % 6;
    e.b = e.q == 6 ? 2 : (e.q < 6 ? e.q & 1 : e.q - 7);
    const int pb = e.q == 6 ? 6 : e.q - e.b;
    e.off = u < 6 ? ((r / 9) * 18 + pb) * 1024 : ((u == 8 ? pb - 6 : pb) * 12 + 2 * (r % 6)) * 1024;
    if (u == 0) {
      if (r < 5) { e.lk = L_W; e.la = 2; e.lb = r; }
      else if (r < 9) { e.lk = L_X; e.la = 8 + r - 5; }
      else if (r < 14) { e.lk = L_W; e.la = 3; e.lb = r - 9; }
      else { e.lk = L_X; e.la = 12 + r - 14; }
    } else if (u <= 2) {
      if (r <= 8 && !(r & 1)) { e.lk = L_W; e.la = u + 3; e.lb = r / 2; }
      else if (r >= 10 && !(r & 1)) { e.lk = L_X; e.la = 12 + 4 * u + (r - 10) / 2; }
    } else if (u <= 5) {
      if (r <= 8 && !(r & 1)) { e.lk = L_W; e.la = u + 3; e.lb = r / 2; }
      else if (u < 5 && r >= 10 && r < 16) { e.lk = L_X; e.la = 24 + 6 * (u - 3) + r - 10; }
      else if (u == 5 && r >= 10 && r < 17) { e.lk = L_R; e.la = r - 10; }
    }
    t.e[i] = e;
  }
  return t;
}
constexpr K4T K4TAB = k4_table();
constexpr int k4_send(int i) { return i == 122 ? 0 : i == 134 ? 1 : i == 146 ? 2 : -1; }
constexpr int k4_younger(int J, int lk, int la, int lb, int pre) {
  int n = pre;
  for (int j = 0; j < J; ++j) {
    const K4E& e = K4TAB.e[j];
    if (k4_send(j) >= 0) ++n;
    if (e.lk) ++n;
    if (e.lk == lk && e.la == la && e.lb == lb) n = 0;
  }
  return n;
}
constexpr int k4_xoff(int j) { return j < 2 ? UNIT + j * 16384 : j == 2 ? 4 * UNIT : 2 * UNIT + (j - 3) * 16384; }
__device__ __forceinline__ f32x4 add4(f32x4 a, f32x4 b) {
  f32x4 r;
#pragma unroll
  for (int i = 0; i < 4; ++i) asm("v_add_f32 %0, %1, %2" : "=v"(r[i]) : "v"(a[i]), "v"(b[i]));
  return r;
}

__global__ __launch_bounds__(512, 2) void k34_mlp(const u16* __restrict__ Wg, const u16* __restrict__ Wu, char* __restrict__ ws,
                                                  u16* __restrict__ out) {
  extern __shared__ __attribute__((aligned(16))) char sm[];
  int wave = __builtin_amdgcn_readfirstlane(threadIdx.x >> 6), bid = blockIdx.x;
  k3_gemm1(Wg, Wu, ws, sm);
  int lane;
  asm volatile("v_mbcnt_lo_u32_b32 %0, -1, 0\n\tv_mbcnt_hi_u32_b32 %0, -1, %0\n\ts_waitcnt vmcnt(0)" : "=v"(lane), "+s"(wave), "+s"(bid), "+s"(ws) :: "memory");
  const int tid = wave * 64 + lane;
  GLBP unsigned* grp = (GLBP unsigned*)(ws + OFF_FLG + ((bid >> 6) * 8 + (bid & 7)) * 512);
  XBAR();
  const int wm = wave & 3, wk = wave >> 2;
  const int xcd = bid & 7, idx = bid >> 3;
  const int kb = xcd & 3;
  const int m128 = (xcd >> 2) * 8 + (idx >> 2), nt = idx & 3;
  const unsigned voff = lane * 16;
  const unsigned lds0 = (unsigned)(uintptr_t)(LDSP char*)sm;
  const char* wsrc = ws + OFF_W2 + (size_t)(kb * 4 + nt) * 324 * 1024 + wave * 4608;
  const unsigned wdst = lds0 + wave * 4608;
  const char* h0 = ws + OFF_H2 + ((size_t)(m128 * 4 + wm) * 288 + (kb * 36 + wk) * 2) * 1024;
  u32x4 H[36];
#define LDH(i) ld_frag<((i) & 1) * 1024, H_SC1>(H[i], h0 + ((i) >> 1) * 4096, voff)
  if (wave == 0) {
    unsigned f = 0;
    if (lane == 0) f = __hip_atomic_fetch_add(grp, 1u, __ATOMIC_RELAXED, __HIP_MEMORY_SCOPE_AGENT) + 1u;
    for (unsigned spins = 0; (unsigned)__builtin_amdgcn_readfirstlane(f) < 8u && spins < (1u << 22); ++spins) {
      __builtin_amdgcn_s_sleep(PSL);
      f = __hip_atomic_load(grp, __ATOMIC_RELAXED, __HIP_MEMORY_SCOPE_AGENT);
    }
  }
  XBAR();
  LDH(0); LDH(1); LDH(2); LDH(3);
  LDH(4); LDH(5); LDH(6); LDH(7);
  f32x4 acc[18];
  u32x4 A[K4_D];
  lptr bu[3];
  LDSP char* xw = (LDSP char*)sm + wave * 2048 + lane * 16;
  lptr xr_ = (lptr)sm + (wave ^ 4) * 2048 + lane * 16;
  const int tile = m128 * 4 + nt;
  __amdgpu_buffer_rsrc_t prs = __builtin_amdgcn_make_buffer_rsrc((void*)(ws + OFF_PART + (size_t)tile * PART_TILE), 0, PART_TILE, 0x00020000);
  f32x4 oth[2], f8;
  const int r0 = m128 * 128 + wm * 32, dA = nt * 9 + 2 * kb + wk, d8 = nt * 9 + 8;
  const char* xfA = ws + OFF_XF + (size_t)(r0 >> 5) * 36864 + dA * 1024;
  const char* xf8 = ws + OFF_XF + (size_t)(r0 >> 5) * 36864 + d8 * 1024 + wk * 8;
  const char* gbp = ws + OFF_MOD + 5 * 576 * 4;
  const char* stp = ws + OFF_ST2 + r0 * 4;
  u32x2 xres[3];
  u32x4 gbv[2];
  unsigned stw[2];
  auto rd = [&](auto jc) {
    constexpr int J = decltype(jc)::value;
    constexpr K4E E = K4TAB.e[J];
    if constexpr (J % 18 == 0 && E.u != 7) {
      constexpr int ST = E.u < 6 ? 1024 : 12288;
      bu[2] = (lptr)sm + (E.u & 3) * UNIT + wk * (E.u < 6 ? 9216 : 1024) + lane * 16;
      bu[1] = bu[2] + wk * ST; bu[0] = bu[2] + (1 - wk) * ST;
      asm volatile("" : "+v"(bu[0]), "+v"(bu[1]), "+v"(bu[2]));
    }
    return *(LDSP const u32x4*)(bu[E.b] + E.off);
  };
  WAIT_VM(4);
  XBAR();
  static_for<0, K4_D>([&](auto jc) { A[decltype(jc)::value] = rd(jc); });
  SB0();
  static_for<0, K4_N>([&](auto jc) {
    constexpr int J = decltype(jc)::value;
    constexpr K4E E = K4TAB.e[J];
    constexpr int R = J - 108;
    if constexpr (J % 18 == 12 && J < 108) {
      wait_vm<k4_younger(J, L_W, E.u + 1, 4, 4)>();
      asm volatile("s_waitcnt lgkmcnt(0)" ::: "memory");
      XBAR();
      TS_STEP(E.u + 2)
    }
    if constexpr (R == 3) { wait_vm<k4_younger(J, L_W, 7, 4, 0)>(); XBAR(); }
    if constexpr (R == 7 || R == 19 || R == 31 || R == 49) {
      constexpr int j = R == 49 ? 4 : R / 12, q = E.q - 1;
      *(LDSP f32x4*)(xw + k4_xoff(j)) = acc[2 * q];
      *(LDSP f32x4*)(xw + k4_xoff(j) + 1024) = acc[2 * q + 1];
      SB0();
    }
    if constexpr (R == 43) {
      if (wk) { *(LDSP f32x4*)(xw + k4_xoff(3)) = acc[12]; f8 = acc[13]; } else { *(LDSP f32x4*)(xw + k4_xoff(3)) = acc[13]; f8 = acc[12]; }
      SB0();
    }
    if constexpr (R == 9 || R == 21 || R == 33 || R == 45 || R == 51) {
      if constexpr (R == 21) wait_vm<k4_younger(J, L_W, 8, 4, 0)>();
      asm volatile("s_waitcnt lgkmcnt(%0)" :: "n"(R < 48 ? 2 : 0) : "memory");
      XBAR();
      if constexpr (R == 9 || R == 33) { TS_STEP(R == 9 ? 8 : 9) }
      constexpr int j = R == 51 ? 4 : R / 12;
      oth[0] = *(LDSP const f32x4*)(xr_ + k4_xoff(j));
      if constexpr (j != 3) oth[1] = *(LDSP const f32x4*)(xr_ + k4_xoff(j) + 1024);
      SB0();
    }
    if constexpr (k4_send(J) >= 0) {
      constexpr int j = k4_send(J);
      const f32x4 s0 = add4(acc[4 * j + 2], oth[0]), s1 = add4(acc[4 * j + 3], oth[1]);
      __builtin_amdgcn_raw_buffer_store_b128((u32x4){pk2(s0[0], s0[1]), pk2(s0[2], s0[3]), pk2(s1[0], s1[1]), pk2(s1[2], s1[3])}, prs, voff,
                                             ((((kb + j + 1) & 3) * 4 + kb) * 8 + wave) * 1024, 16);
      SB0();
    }
    if constexpr (R == 47) {
      f8 = add4(f8, oth[0]);
      if (wm != kb)
        __builtin_amdgcn_raw_buffer_store_b64((u32x2){pk2(f8[0], f8[1]), pk2(f8[2], f8[3])}, prs, lane * 8, 131072 + ((wm * 4 + kb) * 2 + wk) * 512, 16);
      SB0();
    }
    constexpr bool first = E.u < 6 ? E.q == 0 : J < 114;
    if constexpr (first) {
      constexpr int hb = E.hb;
      if constexpr (hb >= 2) wait_vm<k4_younger(J, L_X, 2 * hb + 1, 0, hb == 2 ? 2 : 0)>();
      asm volatile("" : "+v"(H[2 * hb]), "+v"(H[2 * hb + 1]));
      const u32x4 l0 = H[2 * hb], l1 = H[2 * hb + 1];
      H[2 * hb] = (u32x4){l0[0], l0[1], l1[0], l1[1]};
      H[2 * hb + 1] = (u32x4){l0[2], l0[3], l1[2], l1[3]};
      asm volatile("s_nop 1" : "+v"(H[2 * hb]), "+v"(H[2 * hb + 1]));
      SB0();
    }
    const u32x4 a = A[J % K4_D];
    mma16t<(J < 9)>(acc[2 * E.q], a, H[2 * E.hb]);
    if constexpr (J + K4_D < K4_N) A[J % K4_D] = rd(IC<J + K4_D>{});
    SB0();
    mma16t<(J < 9)>(acc[2 * E.q + 1], a, H[2 * E.hb + 1]);
    if constexpr (E.lk == L_X) LDH(E.la);
    if constexpr (E.lk == L_W) dma_piece<E.lb>(wsrc + E.la * UNIT, voff, wdst + (E.la & 3) * UNIT);
    if constexpr (E.lk == L_R) {
      if constexpr (E.la < 3)
        asm volatile("global_load_dwordx2 %0, %1, %2" : "=v"(xres[E.la]) : "v"(voff), "s"(E.la == 2 ? xf8 : xfA + E.la * 8) : "memory");
      else if constexpr (E.la < 5)
        asm volatile("global_load_dwordx4 %0, %1, %2" : "=v"(gbv[E.la - 3]) : "v"(lane & 48), "s"(gbp + (E.la == 3 ? dA : d8) * 64) : "memory");
      else
        asm volatile("global_load_dword %0, %1, %2 sc1" : "=v"(stw[E.la - 5]) : "v"((lane & 15) * 4), "s"(stp + (E.la - 5) * 64) : "memory");
    }
    SB0();
  });
  WAIT_VM(0);
  asm volatile("" : "+v"(xres[0]), "+v"(xres[1]), "+v"(xres[2]), "+v"(gbv[0]), "+v"(gbv[1]), "+v"(stw[0]), "+v"(stw[1]));
  XBAR();
  GLBP unsigned* cnt = (GLBP unsigned*)(ws + OFF_CNT + tile * 128);
  if (tid == 0) __hip_atomic_fetch_add(cnt, 1u, __ATOMIC_RELAXED, __HIP_MEMORY_SCOPE_AGENT);
  MFMA_DRAIN();
  f32x4 fin[3] = {add4(acc[16], oth[0]), add4(acc[17], oth[1]), f8};
  float rs2[3];
#pragma unroll
  for (int i = 0; i < 2; ++i) { const float r = 1.f / hi16(stw[i]); rs2[i] = r * r; }
  rs2[2] = wk ? rs2[1] : rs2[0];
  if (wave == 0) {
    for (unsigned spins = 0; spins < (1u << 22); ++spins) {
      if ((unsigned)__builtin_amdgcn_readfirstlane(__hip_atomic_load(cnt, __ATOMIC_RELAXED, __HIP_MEMORY_SCOPE_AGENT)) >= 4u) break;
      __builtin_amdgcn_s_sleep(1);
    }
  }
  BARRIER();
  u32x4 pp[3];
  u32x2 p8[3];
#pragma unroll
  for (int j = 0; j < 3; ++j) {
    const int src = (kb + 1 + j) & 3;
    pp[j] = __builtin_amdgcn_raw_buffer_load_b128(prs, voff, ((kb * 4 + src) * 8 + wave) * 1024, 16);
    p8[j] = __builtin_amdgcn_raw_buffer_load_b64(prs, lane * 8, 131072 + ((kb * 4 + src) * 2 + wk) * 512, 16);
  }
#pragma unroll
  for (int i = 0; i < 3; ++i) {
    if (i == 2 && wm != kb) break;
    u32x2 o;
#pragma unroll
    for (int e = 0; e < 2; ++e) {
      const unsigned q0 = i < 2 ? pp[0][2 * i + e] : p8[0][e], q1 = i < 2 ? pp[1][2 * i + e] : p8[1][e], q2 = i < 2 ? pp[2][2 * i + e] : p8[2][e];
      float a = ((fin[i][2 * e] + lo16(q0)) + (lo16(q1) + lo16(q2))) * rs2[i];
      float b = ((fin[i][2 * e + 1] + hi16(q0)) + (hi16(q1) + hi16(q2))) * rs2[i];
      bfr2(a, b);
      const unsigned g0 = gbv[i >> 1][2 * e], g1 = gbv[i >> 1][2 * e + 1];
      a *= __uint_as_float(g0); b *= __uint_as_float(g1); bfr2(a, b);
      o[e] = pk2(lo16(xres[i][e]) + a, hi16(xres[i][e]) + b);
    }
    const int m = r0 + (i < 2 ? i : wk) * 16 + (lane & 15);
    *(u32x2*)(out + (size_t)m * D_ + (i < 2 ? dA : d8) * 16 + (lane >> 4) * 4) = o;
  }
}

extern "C" void kernel_launch(void* const* d_in, const int* in_sizes, int n_in, void* d_out, int out_size,
                              void* d_ws, size_t ws_size, hipStream_t stream) {
  const u16* A_q = (const u16*)d_in[0];
  const u16* A_k = (const u16*)d_in[1];
  const u16* A_v = (const u16*)d_in[2];
  const u16* A_sh = (const u16*)d_in[3];
  const u16* A_sk = (const u16*)d_in[4];
  const u16* A_D = (const u16*)d_in[5];
  const u16* Wg = (const u16*)d_in[7];
  const u16* Wu = (const u16*)d_in[8];
  const u16* Wd = (const u16*)d_in[9];
  const u16* cv = (const u16*)d_in[10];
  const u16* wm = (const u16*)d_in[11];
  char* ws = (char*)d_ws;
  (void)hipFuncSetAttribute((const void*)k1_conv, hipFuncAttributeMaxDynamicSharedMemorySize, K1_LDS);
  (void)hipFuncSetAttribute((const void*)k34_mlp, hipFuncAttributeMaxDynamicSharedMemorySize, K4_LDS);
  k0_prep<<<K0_GRID, 256, 0, stream>>>(A_q, A_k, A_v, A_sh, cv, wm, A_sk, ws);
  k1_conv<<<K1_CONV + K1_W, 512, K1_LDS, stream>>>(A_q, A_sk, A_D, Wg, Wu, Wd, ws);
  k34_mlp<<<256, 512, K4_LDS, stream>>>(Wg, Wu, ws, (u16*)d_out);
}
```

```cpp
#include <hip/hip_runtime.h>
#include <hip/hip_bf16.h>
#include <stdint.h>

typedef unsigned short u16;
typedef __attribute__((ext_vector_type(2))) __bf16 bf16x2;
typedef __attribute__((ext_vector_type(8))) __bf16 bf16x8;
typedef __attribute__((ext_vector_type(2))) float f32x2;
typedef __attribute__((ext_vector_type(4))) float f32x4;
typedef __attribute__((ext_vector_type(16))) float f32x16;
typedef __attribute__((ext_vector_type(4))) unsigned u32x4;
typedef __attribute__((ext_vector_type(2))) unsigned u32x2;
typedef __attribute__((ext_vector_type(4))) short s16x4;
#define LDSP __attribute__((address_space(3)))
#define GLBP __attribute__((address_space(1)))

constexpr int S_ = 2048, D_ = 576, F_ = 4608;

constexpr size_t OFF_CNT = 0;
constexpr size_t OFF_MOD = 8192;
constexpr size_t OFF_ST1 = OFF_MOD + 16384;
constexpr size_t OFF_ST2 = OFF_ST1 + 8192;
constexpr size_t OFF_CC  = OFF_ST2 + 8192;
constexpr size_t OFF_UT  = OFF_CC + 32 * 6912;
constexpr size_t OFF_FLG = OFF_UT + (size_t)D_ * S_ * 2;
constexpr size_t OFF_XF  = OFF_FLG + 16384;
constexpr size_t OFF_XS  = OFF_XF + (size_t)D_ * S_ * 2;
constexpr size_t OFF_W2  = OFF_XS + (size_t)D_ * S_ * 2;
constexpr size_t OFF_H2  = OFF_W2 + (size_t)D_ * F_ * 2;
constexpr size_t OFF_PART = OFF_H2 + (size_t)S_ * F_ * 2;
constexpr int PART_TILE = 131072 + 16384;

__device__ __forceinline__ unsigned pk2(float a, float b) {
  f32x2 v = {a, b};
  return __builtin_bit_cast(unsigned, __builtin_convertvector(v, bf16x2));
}
__device__ __forceinline__ float lo16(unsigned u) { return __uint_as_float(u << 16); }
__device__ __forceinline__ float hi16(unsigned u) { return __uint_as_float(u & 0xffff0000u); }
__device__ __forceinline__ float bfr(float a) { return lo16(pk2(a, a)); }
__device__ __forceinline__ void bfr2(float& a, float& b) { unsigned u = pk2(a, b); a = lo16(u); b = hi16(u); }
__device__ __forceinline__ float bf2f(u16 x) { return __uint_as_float(((unsigned)x) << 16); }
__device__ __forceinline__ s16x4 tr_read(const char* p) {
  return __builtin_amdgcn_ds_read_tr16_b64_v4i16((LDSP s16x4*)p);
}
__device__ __forceinline__ u32x4 tr_read2(const char* p0, const char* p1) {
  s16x4 a = tr_read(p0), b = tr_read(p1);
  u32x2 x = __builtin_bit_cast(u32x2, a), y = __builtin_bit_cast(u32x2, b);
  u32x4 r = {x[0], x[1], y[0], y[1]};
  return r;
}

#define BARRIER() { asm volatile("s_waitcnt lgkmcnt(0)" ::: "memory"); __builtin_amdgcn_s_barrier(); asm volatile("" ::: "memory"); }
#define PIN4(a, b, c, d) asm volatile("" : "+v"(a), "+v"(b), "+v"(c), "+v"(d))
typedef LDSP const char* lptr;
__device__ __forceinline__ u32x4 tr_read2l(lptr p0, lptr p1) {
  s16x4 a = __builtin_amdgcn_ds_read_tr16_b64_v4i16((LDSP s16x4*)p0), b = __builtin_amdgcn_ds_read_tr16_b64_v4i16((LDSP s16x4*)p1);
  u32x2 x = __builtin_bit_cast(u32x2, a), y = __builtin_bit_cast(u32x2, b);
  u32x4 r = {x[0], x[1], y[0], y[1]};
  return r;
}

#define CODE_TAIL 1900
#define NT 15
template <int B> __device__ __forceinline__ u32x4 ldv(const void* p) {
  if constexpr ((NT >> B) & 1) return __builtin_nontemporal_load((const u32x4*)p); else return *(const u32x4*)p;
}
__device__ __forceinline__ void wd_load(u32x4 (&wd)[9], const u16* Wd, int pc, int lane) {
#pragma unroll
  for (int i = 0; i < 9; ++i) {
    const int c = i * 64 + lane;
    wd[i] = ldv<3>((const char*)Wd + (size_t)(pc >> 2) * 36864 + (pc & 3) * 288 + (c / 18) * 1152 + (c % 18) * 16);
  }
}
__device__ __forceinline__ void wd_store(const u32x4 (&wd)[9], char* im, char* ws, int pc, int lane) {
#pragma unroll
  for (int i = 0; i < 9; ++i) *(u32x4*)(im + i * 1024 + lane * 16) = wd[i];
  const char* base = im + ((lane >> 4) * 4 + ((lane & 15) >> 2)) * 288 + (lane & 3) * 8;
  const int kb = (pc >> 2) / 36, Tl = (pc >> 2) % 36;
  __amdgpu_buffer_rsrc_t wrs = __builtin_amdgcn_make_buffer_rsrc((void*)(ws + OFF_W2 + (size_t)(kb * 4 + (pc & 3)) * 331776), 0, 331776, 0x00020000);
  asm volatile("s_waitcnt lgkmcnt(0)" ::: "memory");
#pragma unroll
  for (int i = 0; i < 9; ++i) {
    const u32x4 r = tr_read2(base + i * 32, base + i * 32 + 16 * 288);
    const int o = ((i >> 1) - kb) & 3;
    const int pos = i == 8 ? 6 : (o ? 2 * (o - 1) + (i & 1) : 7 + (i & 1));
    __builtin_amdgcn_raw_buffer_store_b128(r, wrs, lane * 16, (Tl < 24 ? Tl * 9 + pos : 216 + pos * 12 + Tl - 24) * 1024, 16);
  }
}
#define K0_AUX 16
constexpr int K0_MOD = 108, K0_KV = 288, K0_ST = 64;
constexpr int K0_GRID = K0_MOD + K0_KV + K0_ST;

__global__ __launch_bounds__(256) void k0_prep(const u16* __restrict__ xq, const u16* __restrict__ kk,
                                               const u16* __restrict__ vv, const u16* __restrict__ shift_k,
                                               const u16* __restrict__ cvec, const u16* __restrict__ w_mod,
                                               const u16* __restrict__ ssm_k, char* __restrict__ ws) {
  __shared__ __attribute__((aligned(16))) char sm[8192];
  const int tid = threadIdx.x, lane = tid & 63, wave = tid >> 6;
  int bid = blockIdx.x;
  if (bid < K0_MOD) {
    if (bid == 0 && tid < 64) *(unsigned*)(ws + OFF_CNT + tid * 128) = 0u;
    if (bid == 9) *(unsigned*)(ws + OFF_FLG + tid * 64) = 0u;
    if (bid >= 1 && bid <= 8) ((unsigned*)(ws + OFF_ST2))[(bid - 1) * 256 + tid] = 0u;
    const int c4 = tid & 3, rg = tid >> 2;
    u32x4 w[9];
    float sc[9];
#pragma unroll
    for (int i = 0; i < 9; ++i) {
      const int row = rg + 64 * i;
      w[i] = ldv<0>(w_mod + (size_t)row * 3456 + bid * 32 + c4 * 8);
      sc[i] = bf2f(cvec[row]);
    }
    float acc[8];
#pragma unroll
    for (int e = 0; e < 8; ++e) acc[e] = 0.f;
#pragma unroll
    for (int i = 0; i < 9; ++i) {
      const float s = bfr(sc[i] / (1.f + __expf(-sc[i])));
#pragma unroll
      for (int e = 0; e < 4; ++e) {
        acc[2 * e] += s * lo16(w[i][e]);
        acc[2 * e + 1] += s * hi16(w[i][e]);
      }
    }
#pragma unroll
    for (int e = 0; e < 8; ++e) {
#pragma unroll
      for (int o = 4; o < 64; o <<= 1) acc[e] += __shfl_xor(acc[e], o);
    }
    float* red = (float*)sm;
    if (lane < 4) {
#pragma unroll
      for (int e = 0; e < 8; ++e) red[wave * 32 + lane * 8 + e] = acc[e];
    }
    __syncthreads();
    if (tid < 32) {
      const float r = (red[tid] + red[32 + tid]) + (red[64 + tid] + red[96 + tid]);
      ((float*)(ws + OFF_MOD))[bid * 32 + tid] = bfr(r);
    }
    return;
  }
  bid -= K0_MOD;
  if (bid < K0_KV) {
    const int cb = bid % 9, tb = bid / 9;
    const int c8 = tid & 7, rp = tid >> 3;
    const int r0 = tb * 64 + rp * 2;
    const int ch0 = cb * 64 + c8 * 8;
    u32x4 kr[5], vr[2], sw[4];
    unsigned warm = 0;
    {
      const char* pc;
      unsigned len;
      asm volatile("s_getpc_b64 %0\n.Lk0_here:\n\ts_mov_b32 %1, .Lk34_tail-.Lk0_here" : "=s"(pc), "=s"(len));
      const unsigned off = (bid >> 3) * 8192 + (tid & 127) * 64;
      const char* wp = tid < 128 ? (const char*)ssm_k + (blockIdx.x & 7) * 294912 + off : pc + off;
      if (tid < 128 || off < len + CODE_TAIL) asm volatile("global_load_dword %0, %1, off" : "+v"(warm) : "v"(wp) : "memory");
    }
#pragma unroll
    for (int i = 0; i < 5; ++i) {
      const int r = r0 - 3 + i;
      const int rc = r < 0 ? 0 : r;
      kr[i] = ldv<1>(kk + (size_t)rc * D_ + ch0);
      if (r < 0) kr[i] = (u32x4){0u, 0u, 0u, 0u};
    }
#pragma unroll
    for (int i = 0; i < 2; ++i) vr[i] = ldv<2>(vv + (size_t)(r0 + i) * D_ + ch0);
#pragma unroll
    for (int i = 0; i < 4; ++i) sw[i] = *(const u32x4*)(shift_k + (size_t)ch0 * 4 + i * 8);
    char* tu = sm;
    const int q = rp >> 2, inner = rp & 3;
#pragma unroll
    for (int e = 0; e < 8; ++e) {
      const unsigned wa = sw[e >> 1][(e & 1) * 2], wb = sw[e >> 1][(e & 1) * 2 + 1];
      const float w0 = lo16(wa), w1 = hi16(wa), w2 = lo16(wb), w3 = hi16(wb);
      float kf[5];
#pragma unroll
      for (int i = 0; i < 5; ++i) kf[i] = (e & 1) ? hi16(kr[i][e >> 1]) : lo16(kr[i][e >> 1]);
      const float v0 = (e & 1) ? hi16(vr[0][e >> 1]) : lo16(vr[0][e >> 1]);
      const float v1 = (e & 1) ? hi16(vr[1][e >> 1]) : lo16(vr[1][e >> 1]);
      float a0 = kf[0] * w3, a1 = kf[1] * w3; bfr2(a0, a1);
      float t0 = kf[1] * w2, t1 = kf[2] * w2; bfr2(t0, t1);
      a0 += t0; a1 += t1; bfr2(a0, a1);
      t0 = kf[2] * w1; t1 = kf[3] * w1; bfr2(t0, t1);
      a0 += t0; a1 += t1; bfr2(a0, a1);
      t0 = kf[3] * w0; t1 = kf[4] * w0; bfr2(t0, t1);
      a0 += t0; a1 += t1; bfr2(a0, a1);
      const unsigned kvp = pk2(a0 * v0, a1 * v1);
      const int off = (c8 * 8 + e) * 128 + ((q ^ c8) * 16) + inner * 4;
      *(unsigned*)(tu + off) = kvp;
    }
    __syncthreads();
    asm volatile("s_waitcnt vmcnt(0)" : "+v"(warm));
    __amdgpu_buffer_rsrc_t urs = __builtin_amdgcn_make_buffer_rsrc((void*)(ws + OFF_UT), 0, D_ * S_ * 2, 0x00020000);
#pragma unroll
    for (int i = 0; i < 2; ++i) {
      const int p = tid + 256 * i, ch = p >> 3, qq = p & 7;
      const int off = ch * 128 + ((qq ^ (ch >> 3)) * 16);
      __builtin_amdgcn_raw_buffer_store_b128(*(const u32x4*)(tu + off), urs, ((cb * 64 + ch) * S_ + tb * 64 + qq * 8) * 2, 0, K0_AUX);
    }
    return;
  }
  bid -= K0_KV;
  {
    const int row = bid * 32 + wave * 8 + (lane >> 3), c = lane & 7;
    u32x4 xr[9];
#pragma unroll
    for (int i = 0; i < 9; ++i) xr[i] = ldv<5>(xq + (size_t)row * D_ + (c + 8 * i) * 8);
    float s = 0.f;
#pragma unroll
    for (int i = 0; i < 9; ++i)
#pragma unroll
      for (int e = 0; e < 4; ++e) s += lo16(xr[i][e]) + hi16(xr[i][e]);
    s += __shfl_xor(s, 1); s += __shfl_xor(s, 2); s += __shfl_xor(s, 4);
    const float mean = s * (1.f / 576.f);
    float ss = 0.f;
#pragma unroll
    for (int i = 0; i < 9; ++i)
#pragma unroll
      for (int e = 0; e < 4; ++e) {
        const float a = lo16(xr[i][e]) - mean, b = hi16(xr[i][e]) - mean;
        ss += a * a + b * b;
      }
    ss += __shfl_xor(ss, 1); ss += __shfl_xor(ss, 2); ss += __shfl_xor(ss, 4);
    const float var = bfr(ss * (1.f / 576.f));
    const float sd = sqrtf(bfr(var + 1e-5f));
    if (c == 0) ((unsigned*)(ws + OFF_ST1))[row] = pk2(mean, sd);
  }
}

constexpr int K1_CONV = 144, K1_W = 108;
constexpr int KC_STRIDE = 4416;
constexpr int CH_BYTES = 4 * KC_STRIDE + 4672;
constexpr int K1_XCH = 4 * CH_BYTES;
constexpr int K1_X1 = K1_XCH + 32768;
constexpr int K1_LDS = K1_X1 + 32768;

#define K1_AUX 16
__global__ __launch_bounds__(512, 2) void k1_conv(const u16* __restrict__ xq, const u16* __restrict__ ssm_k, const u16* __restrict__ ssm_D,
                                                  const u16* __restrict__ Wg, const u16* __restrict__ Wu,
                                                  const u16* __restrict__ Wd, char* __restrict__ ws) {
  extern __shared__ __attribute__((aligned(16))) char sm[];
  const int tid = threadIdx.x, lane = tid & 63, wave = tid >> 6;
  const float* mod = (const float*)(ws + OFF_MOD);
  int bid = blockIdx.x;
  if (bid < K1_CONV) {
    const int cg = (bid & 7) * 18 + (bid >> 3);
    const int cl = wave >> 1, w2 = wave & 1;
    const int d = cg * 4 + cl;
    char* chb = sm + cl * CH_BYTES;
    char* ub = chb + 4 * KC_STRIDE;
    const int t128 = tid & 127;
    const char* krow = (const char*)ssm_k + (size_t)d * 4096;
    const char* urow = ws + OFF_UT + (size_t)d * 4096;
    u32x4 ka[2], ua[2];
    u32x2 kb[2];
#pragma unroll
    for (int i = 0; i < 2; ++i) {
      const int c = t128 + 128 * i;
      ka[i] = *(const u32x4*)(krow + c * 16);
      const int cn = c < 255 ? c + 1 : 255;
      kb[i] = *(const u32x2*)(krow + cn * 16);
      if (c == 255) kb[i] = (u32x2){0u, 0u};
      ua[i] = *(const u32x4*)(urow + c * 16);
    }
    const int li = lane & 15, g = lane >> 4;
    const float Dd = bf2f(ssm_D[d]);
    {
      const u32x4 z = {0u, 0u, 0u, 0u};
      if (t128 < 32) *(u32x4*)(ub + t128 * 16) = z;
      else if (t128 < 32 + 12) {
        const int i = t128 - 32;
        *(u32x4*)(chb + (i / 3) * KC_STRIDE + (i % 3) * 16) = z;
      } else if (t128 < 32 + 12 + 16) {
        const int i = t128 - 44;
        *(u32x4*)(chb + (i >> 2) * KC_STRIDE + 64 + 4096 + (i & 3) * 16) = z;
      } else if (t128 < 64) {
        *(u32x4*)(ub + 4608 + (t128 - 60) * 16) = z;
      }
    }
#pragma unroll
    for (int i = 0; i < 2; ++i) {
      const int c = t128 + 128 * i;
      const unsigned w0 = ka[i][0], w1 = ka[i][1], w2_ = ka[i][2], w3 = ka[i][3], w4 = kb[i][0], w5 = kb[i][1];
      const unsigned a0 = __builtin_amdgcn_alignbit(w1, w0, 16), a1 = __builtin_amdgcn_alignbit(w2_, w1, 16),
                     a2 = __builtin_amdgcn_alignbit(w3, w2_, 16), a3 = __builtin_amdgcn_alignbit(w4, w3, 16),
                     a4 = __builtin_amdgcn_alignbit(w5, w4, 16);
      char* p = chb + 64 + c * 16;
      *(u32x4*)(p) = ka[i];
      *(u32x4*)(p + KC_STRIDE) = (u32x4){a0, a1, a2, a3};
      *(u32x4*)(p + 2 * KC_STRIDE) = (u32x4){w1, w2_, w3, w4};
      *(u32x4*)(p + 3 * KC_STRIDE) = (u32x4){a1, a2, a3, a4};
      *(u32x4*)(ub + 512 + c * 16) = ua[i];
      if (c == 0) {
        *(u32x4*)(p - 16) = (u32x4){0u, 0u, 0u, 0u};
        *(u32x4*)(p - 16 + KC_STRIDE) = (u32x4){0u, 0u, 0u, w0 << 16};
        *(u32x4*)(p - 16 + 2 * KC_STRIDE) = (u32x4){0u, 0u, 0u, w0};
        *(u32x4*)(p - 16 + 3 * KC_STRIDE) = (u32x4){0u, 0u, w0 << 16, a0};
      }
    }
    u32x2 xv[4];
    unsigned st[4];
#pragma unroll
    for (int i = 0; i < 4; ++i) {
      const int s = (wave * 8 + (i >> 1) * 4 + g) * 32 + (i & 1) * 16 + li;
      st[i] = ((const unsigned*)(ws + OFF_ST1))[s];
    }
    f32x4 sa = *(const f32x4*)(mod + cg * 4), ca = *(const f32x4*)(mod + 576 + cg * 4);
    f32x4 ga = *(const f32x4*)(mod + 1152 + cg * 4), cb = *(const f32x4*)(mod + 2304 + cg * 4);
    BARRIER();
    const int q = li >> 2, p4 = li & 3;
    const char* abase = chb + ((4 - q) & 3) * KC_STRIDE + 2 * (32 - 8 * g - (q ? 4 : 0) + 4 * p4) - 8;
    const char* bbase = ub + 2 * (256 + 16 * li + 8 * g);
    f32x4 acc[8];
#pragma unroll
    for (int b = 0; b < 8; ++b) acc[b] = (f32x4){0.f, 0.f, 0.f, 0.f};
    u32x2 hp[4], tp[4];
    auto hfun = [&](int i) {
      asm volatile("" : "+v"(st[i]), "+v"(xv[i]));
      const float mu = lo16(st[i]), rs = __builtin_amdgcn_rcpf(hi16(st[i]));
#pragma unroll
      for (int e = 0; e < 2; ++e) {
        float h0 = lo16(xv[i][e]) - mu, h1 = hi16(xv[i][e]) - mu; bfr2(h0, h1);
        h0 *= rs; h1 *= rs; bfr2(h0, h1);
        h0 *= ca[2 * e]; h1 *= ca[2 * e + 1]; bfr2(h0, h1);
        hp[i][e] = pk2(h0 + sa[2 * e], h1 + sa[2 * e + 1]);
      }
      asm volatile("" : "+v"(hp[i]));
    };
    const char* ap = abase + 64 * (1 + w2);
    const char* bp = bbase - 64 * (1 + w2) - 384;
    bf16x8 A[2][8], B[2][8];
#pragma unroll
    for (int i = 0; i < 8; ++i) {
      A[0][i] = __builtin_bit_cast(bf16x8, tr_read2(ap + 512 * i + 8, ap + 512 * i));
      B[0][i] = *(const bf16x8*)(bp + 384 + 512 * i);
    }
    __builtin_amdgcn_sched_barrier(0);
#pragma unroll
    for (int it = 0; it < 4; ++it) {
#pragma unroll
      for (int b = 0; b < 8; ++b) {
        if (it < 3) {
          A[(it + 1) & 1][b] = __builtin_bit_cast(bf16x8, tr_read2(ap + 128 * (it + 1) + 512 * b + 8, ap + 128 * (it + 1) + 512 * b));
          B[(it + 1) & 1][b] = *(const bf16x8*)(bp + 384 - 128 * (it + 1) + 512 * b);
        }
        if ((it == 0 && (b == 2 || b == 6)) || ((it == 1 || it == 2) && b == 2)) {
          const int i = it ? it + 1 : b == 6;
          xv[i] = *(const u32x2*)(xq + (size_t)((wave * 8 + (i >> 1) * 4 + g) * 32 + (i & 1) * 16 + li) * D_ + cg * 4);
        }
        if (it == 1 && b >= 3 && b < 7) tp[b - 3] = *(const u32x2*)(ub + 512 + (256 * (4 * w2 + b - 3) + 16 * li + 4 * g) * 2);
        if (it == 1 && b >= 4) {
          const u32x2 uu = tp[b - 4];
          tp[b - 4] = (u32x2){pk2(lo16(uu[0]) * Dd, hi16(uu[0]) * Dd), pk2(lo16(uu[1]) * Dd, hi16(uu[1]) * Dd)};
          asm volatile("" : "+v"(tp[b - 4]));
        }
        if (it == 2 && b == 6) {
          asm volatile("" : "+v"(ca), "+v"(cb));
#pragma unroll
          for (int e = 0; e < 4; ++e) { ca[e] = bfr(1.f + ca[e]); cb[e] = bfr(1.f + cb[e]); }
          asm volatile("" : "+v"(ca), "+v"(cb));
        }
        if (it == 2 && b == 7) hfun(0);
        if (it == 3 && b >= 5) hfun(b - 4);
#pragma unroll
        for (int qq = 0; qq <= b; ++qq)
          acc[b] = __builtin_amdgcn_mfma_f32_16x16x32_bf16(A[it & 1][qq], B[it & 1][b - qq], acc[b], 0, 0, 0);
        __builtin_amdgcn_sched_barrier(0);
      }
    }
    char* xch = sm + K1_XCH;
    f32x4 fin[4];
    if (w2 == 0) {
#pragma unroll
      for (int i = 0; i < 4; ++i) { *(f32x4*)(xch + wave * 4096 + i * 1024 + lane * 16) = acc[4 + i]; fin[i] = acc[i]; }
    } else {
#pragma unroll
      for (int i = 0; i < 4; ++i) { *(f32x4*)(xch + wave * 4096 + i * 1024 + lane * 16) = acc[i]; fin[i] = acc[4 + i]; }
    }
    {
      const bf16x8 A0 = __builtin_bit_cast(bf16x8, tr_read2(abase + 8, abase));
#pragma unroll
      for (int i = 0; i < 4; ++i) {
        const bf16x8 Bd = *(const bf16x8*)(bbase + 512 * (4 * w2 + i));
        fin[i] = __builtin_amdgcn_mfma_f32_16x16x32_bf16(A0, Bd, fin[i], 0, 0, 0);
      }
    }
    PIN4(fin[0], fin[1], fin[2], fin[3]);
    __syncthreads();
    char* ys = sm + K1_X1;
#pragma unroll
    for (int i = 0; i < 4; ++i) {
      const f32x4 o = *(const f32x4*)(xch + (wave ^ 1) * 4096 + i * 1024 + lane * 16);
      u32x2 y;
#pragma unroll
      for (int e = 0; e < 2; ++e) {
        float y0 = fin[i][2 * e] + o[2 * e], y1 = fin[i][2 * e + 1] + o[2 * e + 1]; bfr2(y0, y1);
        y[e] = pk2(y0 + lo16(tp[i][e]), y1 + hi16(tp[i][e]));
      }
      *(u32x2*)(ys + cl * 4128 + (256 * (4 * w2 + i) + 16 * li + 4 * g) * 2) = y;
    }
    __syncthreads();
    __amdgpu_buffer_rsrc_t xrs = __builtin_amdgcn_make_buffer_rsrc((void*)(ws + OFF_XF), 0, 2 * D_ * S_ * 2, 0x00020000);
#pragma unroll
    for (int j = 0; j < 2; ++j) {
      const int mt = wave * 8 + j * 4 + g;
      const char* yp = ys + q * 4128 + mt * 64 + p4 * 8;
      const u32x4 y = tr_read2(yp, yp + 32);
      u32x4 o1, o2;
#pragma unroll
      for (int e = 0; e < 4; ++e) {
        const unsigned hh = hp[2 * j + (e >> 1)][e & 1], xx = xv[2 * j + (e >> 1)][e & 1];
        const int c = (e & 1) * 2;
        float y0 = lo16(y[e]) * lo16(hh), y1 = hi16(y[e]) * hi16(hh); bfr2(y0, y1);
        y0 *= ga[c]; y1 *= ga[c + 1]; bfr2(y0, y1);
        const unsigned r = pk2(lo16(xx) + y0, hi16(xx) + y1);
        o1[e] = r;
        o2[e] = pk2(lo16(r) * cb[c], hi16(r) * cb[c + 1]);
      }
      const int off = mt * 36864 + cg * 256 + li * 16;
      __builtin_amdgcn_raw_buffer_store_b128(o2, xrs, off, D_ * S_ * 2, K1_AUX);
      __builtin_amdgcn_raw_buffer_store_b128(o1, xrs, off, 0, K1_AUX);
    }
    return;
  }
  bid -= K1_CONV;
  {
    const int cbk = bid % 36, kt = bid / 36;
    const int mat = cbk / 18, cblk = cbk % 18;
    const u16* W = mat ? Wu : Wg;
    const int p = tid & 31, rg = tid >> 5;
    u32x4 w[12];
    float cs[12], sb[12];
#pragma unroll
    for (int i = 0; i < 12; ++i) {
      const int k = kt * 192 + rg + 16 * i;
      w[i] = ldv<4>(W + (size_t)k * F_ + cblk * 256 + p * 8);
    }
    __builtin_amdgcn_sched_barrier(0);
#pragma unroll
    for (int i = 0; i < 12; ++i) {
      const int k = kt * 192 + rg + 16 * i;
      cs[i] = mod[4 * 576 + k];
      sb[i] = mod[3 * 576 + k];
    }
    const int pc = __builtin_amdgcn_readfirstlane(wave) * K1_W + bid;
    u32x4 wd[9];
    if (pc < 576) wd_load(wd, Wd, pc, lane);
    __builtin_amdgcn_sched_barrier(0);
    float a1[8], a2[8];
#pragma unroll
    for (int e = 0; e < 8; ++e) { a1[e] = 0.f; a2[e] = 0.f; }
#pragma unroll
    for (int i = 0; i < 12; ++i) {
      const float c1 = bfr(1.f + cs[i]);
#pragma unroll
      for (int e = 0; e < 4; ++e) {
        const float l = lo16(w[i][e]), h = hi16(w[i][e]);
        a1[2 * e] += c1 * l; a1[2 * e + 1] += c1 * h;
        a2[2 * e] += sb[i] * l; a2[2 * e + 1] += sb[i] * h;
      }
    }
    float* red = (float*)(sm + 110592);
#pragma unroll
    for (int e = 0; e < 8; ++e) { a1[e] += __shfl_xor(a1[e], 32); a2[e] += __shfl_xor(a2[e], 32); }
    if (lane < 32) {
      *(f32x4*)(red + wave * 512 + p * 8) = (f32x4){a1[0], a1[1], a1[2], a1[3]};
      *(f32x4*)(red + wave * 512 + p * 8 + 4) = (f32x4){a1[4], a1[5], a1[6], a1[7]};
    } else {
      *(f32x4*)(red + wave * 512 + 256 + p * 8) = (f32x4){a2[0], a2[1], a2[2], a2[3]};
      *(f32x4*)(red + wave * 512 + 256 + p * 8 + 4) = (f32x4){a2[4], a2[5], a2[6], a2[7]};
    }
    BARRIER();
    {
      float r = 0.f;
#pragma unroll
      for (int i = 0; i < 8; ++i) r += red[i * 512 + tid];
      const int which = tid >> 8, col = tid & 255;
      const int ft = cblk * 16 + (col >> 4);
      ((float*)(ws + OFF_CC))[(ft / 9) * 1728 + kt * 576 + which * 288 + (ft % 9) * 32 + mat * 16 + (col & 15)] = r;
    }
    if (pc < 576) wd_store(wd, sm + wave * 9216, ws, pc, lane);
  }
}

constexpr int UNIT = 36864;

__device__ __forceinline__ void dma_unit(const char* gbase  , unsigned voff, unsigned lds_dst  ) {
  unsigned keep;
  asm volatile(
      "s_mov_b32 %0, m0\n\t"
      "s_mov_b32 m0, %3\n\t"
      "s_mov_b32 exec_hi, 0\n\t"
      "global_load_lds_dwordx4 %1, %2\n\t"
      "s_mov_b32 exec_hi, -1\n\t"
      "global_load_lds_dwordx4 %1, %2 offset:512\n\t"
      "global_load_lds_dwordx4 %1, %2 offset:1536\n\t"
      "global_load_lds_dwordx4 %1, %2 offset:2560\n\t"
      "global_load_lds_dwordx4 %1, %2 offset:3584\n\t"
      "s_mov_b32 m0, %0"
      : "=&s"(keep)
      : "v"(voff), "s"(gbase), "s"(lds_dst)
      : "memory");
}
template <int OFF, bool SC1 = false>
__device__ __forceinline__ void ld_frag(u32x4& dst, const char* gbase  , unsigned voff) {
  if constexpr (SC1) asm volatile("global_load_dwordx4 %0, %1, %2 offset:%3 sc1" : "=v"(dst) : "v"(voff), "s"(gbase), "i"(OFF) : "memory");
  else asm volatile("global_load_dwordx4 %0, %1, %2 offset:%3" : "=v"(dst) : "v"(voff), "s"(gbase), "i"(OFF) : "memory");
}
#define PSL 1
#define H_SC1 1
#define TS_STEP(i)
#define H2_AUX 16
#define WAIT_VM(n) asm volatile("s_waitcnt vmcnt(" #n ")" ::: "memory")

constexpr int K3_UP = 18432;
constexpr int K3_CCRAW = 4 * UNIT;
constexpr int K3_CC = K3_CCRAW + 6912;
constexpr int K3_LDS = K3_CC + 4608;
constexpr int K3_D = 6;

__device__ __forceinline__ void dma_rows(const char* gbase  , const unsigned (&vo)[5], unsigned lds_dst  ) {
  asm volatile(
      "s_mov_b32 m0, %6\n\t"
      "s_mov_b32 exec_hi, 0\n\t"
      "global_load_lds_dwordx4 %0, %5\n\t"
      "s_mov_b32 exec_hi, -1\n\t"
      "global_load_lds_dwordx4 %1, %5 offset:512\n\t"
      "global_load_lds_dwordx4 %2, %5 offset:1536\n\t"
      "global_load_lds_dwordx4 %3, %5 offset:2560\n\t"
      "global_load_lds_dwordx4 %4, %5 offset:3584"
      :
      : "v"(vo[0]), "v"(vo[1]), "v"(vo[2]), "v"(vo[3]), "v"(vo[4]), "s"(gbase), "s"(lds_dst)
      : "memory");
}
#define SB0() __builtin_amdgcn_sched_barrier(0)
#define XBAR() { SB0(); asm volatile("s_barrier" ::: "memory"); SB0(); }

template <int V> struct IC { static constexpr int value = V; };
template <int I0, int I1, class F>
__device__ __forceinline__ void static_for(F&& f) {
  if constexpr (I0 < I1) { f(IC<I0>{}); static_for<I0 + 1, I1>(f); }
}
template <int J>
__device__ __forceinline__ void dma_piece(const char* gbase  , unsigned vo, unsigned lds_dst  ) {
  if constexpr (J == 0)
    asm volatile("s_mov_b32 m0, %2\n\ts_mov_b32 exec_hi, 0\n\tglobal_load_lds_dwordx4 %0, %1\n\ts_mov_b32 exec_hi, -1"
                 :: "v"(vo), "s"(gbase), "s"(lds_dst) : "memory");
  else
    asm volatile("s_mov_b32 m0, %2\n\tglobal_load_lds_dwordx4 %0, %1 offset:%3"
                 :: "v"(vo), "s"(gbase), "s"(lds_dst), "i"(1024 * J - 512) : "memory");
}
__device__ __forceinline__ f32x4 mma16(u32x4 a, u32x4 b, f32x4 c) {
  return __builtin_amdgcn_mfma_f32_16x16x32_bf16(__builtin_bit_cast(bf16x8, a), __builtin_bit_cast(bf16x8, b), c, 0, 0, 0);
}
template <bool Z>
__device__ __forceinline__ void mma16t(f32x4& c, u32x4 a, u32x4 b) {
  if constexpr (Z) asm volatile("v_mfma_f32_16x16x32_bf16 %0, %1, %2, 0" : "=v"(c) : "v"(a), "v"(b));
  else asm volatile("v_mfma_f32_16x16x32_bf16 %0, %1, %2, %0" : "+v"(c) : "v"(a), "v"(b));
}
#define MFMA_DRAIN() { asm volatile("s_nop 7\n\ts_nop 7" ::: "memory"); SB0(); }
__device__ __forceinline__ float relu(float a) { return __int_as_float(max(__float_as_int(a), 0)); }
__device__ __forceinline__ u32x2 reglu(f32x4 g, f32x4 u) {
  float p[4];
#pragma unroll
  for (int i = 0; i < 4; ++i) asm("v_mul_f32 %0, %1, %2" : "=v"(p[i]) : "v"(relu(g[i])), "v"(u[i]));
  return (u32x2){pk2(p[0], p[1]), pk2(p[2], p[3])};
}

enum { L_NONE = 0, L_X, L_W, L_CC, L_POLL, L_R };
struct K3E {
  int pos, u, k32, tm, off;
  int epi;
  int lk, la, lb;
};
constexpr int K3_N = 342;
struct K3T { K3E e[K3_N]; };
constexpr K3T k3_table() {
  K3T t{};
  int n = 0;
  for (int i = 0; i < 324; ++i) {
    const int u = i / 36, r = i % 36;
    K3E e{};
    e.pos = i; e.u = u; e.epi = -1;
    if (u < 6) {
      e.k32 = 2 * u + r / 18; e.tm = r % 18;
      e.off = (e.tm & 1) * K3_UP + (r / 18) * 9216 + (e.tm >> 1) * 32;
    } else {
      e.k32 = 12 + (r % 12) / 2; e.tm = ((u - 6) * 3 + r / 12) * 2 + (r & 1);
      e.off = (r & 1) * K3_UP + ((r % 12) / 2) * 3072 + (r / 12) * 32;
      if (i > 228 && r % 12 == 2) e.epi = (e.tm >> 1) - 1;
    }
    if (u == 0) {
      if (r < 5) { e.lk = L_W; e.la = 1; e.lb = r; }
      else if (r == 5) e.lk = L_CC;
      else if (r >= 8 && r <= 20 && r % 3 == 2) { e.lk = L_W; e.la = 2; e.lb = (r - 8) / 3; }
      else if (r >= 23 && r <= 32 && r % 3 == 2) { e.lk = L_X; e.la = 8 + (r - 23) / 3; }
    } else if (u == 5) {
      if (r < 4) { e.lk = L_X; e.la = 32 + r; }
      else if (r >= 6 && r <= 18 && r % 3 == 0) { e.lk = L_W; e.la = 7; e.lb = (r - 6) / 3; }
      else if (r == 21) e.lk = L_POLL;
    } else if (u <= 6) {
      if (r <= 12 && r % 3 == 0) { e.lk = L_W; e.la = u + 2; e.lb = r / 3; }
      else if (u < 4 && r >= 15 && r <= 24 && r % 3 == 0) { e.lk = L_X; e.la = 8 + 4 * u + (r - 15) / 3; }
      else if (u == 4 && r >= 15 && r <= 29 && (r & 1)) { e.lk = L_X; e.la = 24 + (r - 15) / 2; }
    } else if (r <= 16 && r % 4 == 0) { e.lk = L_R; e.la = u - 7; e.lb = r / 4; }
    t.e[n++] = e;
    if (u >= 6 && r % 12 == 11) {
      K3E c{};
      c.pos = -1; c.epi = -1;
      c.tm = e.tm - 1; c.off = c.tm * 256; t.e[n++] = c;
      c.tm = e.tm; c.off = c.tm * 256; t.e[n++] = c;
    }
  }
  return t;
}
constexpr K3T K3TAB = k3_table();
constexpr int k3_younger(int J, int lk, int la, int lb) {
  int n = 0;
  for (int j = 0; j < J; ++j) {
    const K3E& e = K3TAB.e[j];
    if (e.epi >= 0) ++n;
    if (e.lk) n += e.lk == L_POLL ? 2 : 1;
    if (e.lk == lk && e.la == la && e.lb == lb) n = 0;
  }
  return n;
}
template <int N> __device__ __forceinline__ void wait_vm() { asm volatile("s_waitcnt vmcnt(%0)" :: "n"(N) : "memory"); }

__device__ __forceinline__ void k3_gemm1(const u16* __restrict__ Wg, const u16* __restrict__ Wu, char* __restrict__ ws, char* sm) {
  const int tid = threadIdx.x, lane = tid & 63;
  const int wave = __builtin_amdgcn_readfirstlane(tid >> 6);
  const int bid = blockIdx.x;
  const int xcd = bid & 7, idx = bid >> 3;
  const int m256 = (xcd >> 2) * 4 + (idx >> 3), sl = (xcd & 3) * 8 + (idx & 7);
  const int mt = m256 * 8 + wave;
  const unsigned voff = lane * 16;
  const unsigned lds0 = (unsigned)(uintptr_t)(LDSP char*)sm;
  const char* wA = (const char*)((wave & 4) ? Wu : Wg) + (size_t)(wave & 3) * 16 * 9216 + sl * 288;
  const char* wB = (const char*)((wave & 4) ? Wu : Wg) + (size_t)(384 + (wave & 3) * 48) * 9216 + sl * 288;
  const unsigned wdst = lds0 + wave * 4608;
  unsigned vo[5];
#pragma unroll
  for (int j = 0; j < 5; ++j) {
    const unsigned off = j ? 1024 * j - 512 : 0, c = off / 16 + lane;
    vo[j] = (c / 18) * 9216 + (c % 18) * 16 - off;
  }
  const char* xsrc = ws + OFF_XS + (size_t)mt * 36864;
  u32x4 X[36];
#define LDX(i) ld_frag<((i) & 3) * 1024>(X[i], xsrc + ((i) >> 2) * 4096, voff)
#define SWP(p, i, q, j) { unsigned a_ = p[i], c_ = q[j]; asm volatile("v_swap_b32 %0, %1" : "+v"(a_), "+v"(c_)); p[i] = a_; q[j] = c_; \
  asm volatile("" : "+v"(p), "+v"(q)); }
#define FIX2(i) { SWP(X[i], 2, X[(i) + 1], 0) SWP(X[i], 3, X[(i) + 1], 1) }
#define FIX4(i) { PIN4(X[i], X[(i) + 1], X[(i) + 2], X[(i) + 3]); FIX2(i) FIX2((i) + 2) \
  asm volatile("s_nop 1" : "+v"(X[i]), "+v"(X[(i) + 1]), "+v"(X[(i) + 2]), "+v"(X[(i) + 3])); }
#define LDX4(i) { LDX(i); LDX((i) + 1); LDX((i) + 2); LDX((i) + 3); }
  dma_rows(wA, vo, wdst + 3 * UNIT);
  LDX4(0)
  XBAR();
  LDX4(4)
  u32x4 sx[9];
  if (wave == 7) {
    const char* sb = ws + OFF_XF + (size_t)(m256 * 8 + (sl >> 2)) * 36864 + (sl & 3) * 64;
    const unsigned so = (lane >> 2) * 256 + (lane & 3) * 16;
#pragma unroll
    for (int i = 0; i < 9; ++i) ld_frag<0>(sx[i], sb + i * 4096, so);
    WAIT_VM(13);
  } else {
    WAIT_VM(4);
  }
  const char* csrc = ws + OFF_CC + (size_t)sl * 6912 + wave * 864;
  const char* w2src = ws + OFF_W2 + (size_t)((xcd & 3) * 4 + (idx & 3)) * 324 * 1024 + wave * 4608;
  const unsigned cdst = lds0 + K3_CCRAW + wave * 864;
  f32x4 acc[36];
  __amdgpu_buffer_rsrc_t hrs = __builtin_amdgcn_make_buffer_rsrc((void*)(ws + OFF_H2 + (size_t)mt * 294912), 0, 294912, 0x00020000);
  const int rw = (lane >> 4) * 4 + ((lane & 15) >> 2);
  lptr bu = (lptr)sm;
  lptr cp = (lptr)sm + K3_CC + (lane & 15) * 16;
  u32x4 A[K3_D];
  unsigned stw[2];
  u32x4 bc[2];
  const char* stp = ws + OFF_ST2 + (size_t)mt * 128;
  auto rd = [&](auto jc, auto hc) -> u32x2 {
    constexpr int H = decltype(hc)::value;
    constexpr K3E E = K3TAB.e[decltype(jc)::value];
    if constexpr (E.pos >= 0) {
      constexpr int RS = E.u < 6 ? 288 : 96;
      if constexpr (E.pos % 36 == 0 && H == 0) { bu = (lptr)sm + ((E.u + 3) & 3) * UNIT + rw * RS + (lane & 3) * 8; asm volatile("" : "+v"(bu)); }
      return __builtin_bit_cast(u32x2, __builtin_amdgcn_ds_read_tr16_b64_v4i16((LDSP s16x4*)(bu + E.off + H * 16 * RS)));
    } else {
      return *(LDSP const u32x2*)(cp + E.off + H * 8);
    }
  };
  auto epi = [&](auto tc) {
    constexpr int T = decltype(tc)::value, c = T * 4;
    const u32x2 h0 = reglu(acc[c], acc[c + 2]), h1 = reglu(acc[c + 1], acc[c + 3]);
    __builtin_amdgcn_raw_buffer_store_b128((u32x4){h0[0], h0[1], h1[0], h1[1]}, hrs, voff, (sl * 9 + T) * 1024, H2_AUX);
    SB0();
  };
#define POLL() asm volatile("global_load_dword %0, %2, %3 sc1\n\tglobal_load_dword %1, %2, %3 offset:64 sc1" \
                            : "=&v"(stw[0]), "=&v"(stw[1]) : "v"((lane & 15) * 4), "s"(stp) : "memory")
  FIX4(0)
  XBAR();
  static_for<0, K3_D>([&](auto jc) {
    const u32x2 n0 = rd(jc, IC<0>{}), n1 = rd(jc, IC<1>{});
    A[decltype(jc)::value] = (u32x4){n0[0], n0[1], n1[0], n1[1]};
  });
  if (wave == 7) {
    WAIT_VM(0);
    PIN4(sx[0], sx[1], sx[2], sx[3]); PIN4(sx[4], sx[5], sx[6], sx[7]); asm volatile("" : "+v"(sx[8]));
    const bf16x2 ones = __builtin_bit_cast(bf16x2, 0x3f803f80u);
    float s1[2] = {0.f, 0.f}, s2[2] = {0.f, 0.f};
#pragma unroll
    for (int i = 0; i < 9; ++i)
#pragma unroll
      for (int e = 0; e < 4; ++e) {
        const unsigned xu = sx[i][e];
        const bf16x2 xx = __builtin_bit_cast(bf16x2, xu);
        s1[e >> 1] = __builtin_amdgcn_fdot2_f32_bf16(xx, ones, s1[e >> 1], false);
        s2[e >> 1] = __builtin_amdgcn_fdot2_f32_bf16(xx, xx, s2[e >> 1], false);
      }
    const bool up = lane & 4;
    float a = (up ? s1[1] : s1[0]) + __shfl_xor(up ? s1[0] : s1[1], 4), b = (up ? s2[1] : s2[0]) + __shfl_xor(up ? s2[0] : s2[1], 4);
#pragma unroll
    for (int o = 8; o < 64; o <<= 1) { a += __shfl_xor(a, o); b += __shfl_xor(b, o); }
    const float mean = a * (1.f / 576.f);
    const float var = fmaxf(b * (1.f / 576.f) - mean * mean, 0.f);
    const unsigned w = pk2(mean, sqrtf(bfr(bfr(var) + 1e-5f)));
    if (lane < 8)
      __hip_atomic_store((GLBP unsigned*)(ws + OFF_ST2) + m256 * 256 + (sl >> 2) * 32 + (sl & 3) * 4 + (lane & 3) + (lane & 4) * 4, w, __ATOMIC_RELAXED, __HIP_MEMORY_SCOPE_AGENT);
  }
  SB0();
  static_for<0, K3_N>([&](auto jc) {
    constexpr int J = decltype(jc)::value;
    constexpr K3E E = K3TAB.e[J];
    constexpr int I = E.pos;
    if constexpr (I >= 0 && I % 36 == 30 && I < 300) {
      wait_vm<k3_younger(J, I == 30 ? L_CC : L_W, I == 30 ? 0 : E.u + 1, I == 30 ? 0 : 4)>();
      XBAR();
      TS_STEP(E.u + 2)
    }
    if constexpr (I == 32) {
      if (tid < 288) {
        const float* rp = (const float*)(sm + K3_CCRAW) + tid;
        const float c1 = rp[0] + rp[576] + rp[1152], c2 = rp[288] + rp[864] + rp[1440];
        const float h1 = bfr(c1), r1 = c1 - h1, m1 = bfr(r1), h2 = bfr(c2), r2 = c2 - h2, m2 = bfr(r2);
        *(u32x4*)(sm + K3_CC + tid * 16) = (u32x4){pk2(h1, m1), pk2(r1 - m1, h2), pk2(m2, r2 - m2), 0u};
      }
      SB0();
    }
    if constexpr (I > 0 && (I <= 216 ? I % 36 == 0 : (I == 220 || I == 224))) {
      constexpr int x0 = E.k32 * 2;
      wait_vm<k3_younger(J, L_X, x0 + 3, 0)>();
      FIX4(x0)
      SB0();
    }
    if constexpr (I == 130) {
#pragma unroll
      for (int j = 0; j < 5; ++j) {
        const unsigned off = j ? 1024 * j - 512 : 0, c = off / 16 + lane;
        vo[j] = (c / 6) * 9216 + (c % 6) * 16 - off;
      }
      SB0();
    }
    if constexpr (I == 226) {
      wait_vm<k3_younger(J, L_POLL, 0, 0)>();
      asm volatile("" : "+v"(stw[0]), "+v"(stw[1]));
      for (unsigned spins = 0; __builtin_amdgcn_ballot_w64(stw[0] == 0u || stw[1] == 0u) != 0ull && spins < (1u << 20); ++spins) {
        __builtin_amdgcn_s_sleep(2);
        POLL(); WAIT_VM(0); asm volatile("" : "+v"(stw[0]), "+v"(stw[1]));
      }
#pragma unroll
      for (int m = 0; m < 2; ++m) {
        const unsigned w = lane < 16 ? (stw[m] ^ 0x8000u) : 0u;
        const unsigned nm = w & 0xffffu, sd = w >> 16;
        bc[m] = (u32x4){nm | (nm << 16), w, sd | (sd << 16), 0u};
      }
      asm volatile("" : "+v"(bc[0]), "+v"(bc[1]));
      SB0();
    }
    if constexpr (E.epi >= 0) epi(IC<E.epi>{});
    const u32x4 a = A[J % K3_D];
    constexpr bool Z = I >= 0 && I < 18;
    u32x2 n0, n1;
    if constexpr (I >= 0) mma16t<Z>(acc[2 * E.tm], a, X[E.k32 * 2]); else mma16t<false>(acc[2 * E.tm], a, bc[0]);
    if constexpr (J + K3_D < K3_N) n0 = rd(IC<J + K3_D>{}, IC<0>{});
    SB0();
    if constexpr (I >= 0) mma16t<Z>(acc[2 * E.tm + 1], a, X[E.k32 * 2 + 1]); else mma16t<false>(acc[2 * E.tm + 1], a, bc[1]);
    if constexpr (J + K3_D < K3_N) { n1 = rd(IC<J + K3_D>{}, IC<1>{}); A[J % K3_D] = (u32x4){n0[0], n0[1], n1[0], n1[1]}; }
    if constexpr (E.lk == L_X) LDX(E.la);
    if constexpr (E.lk == L_W)
      dma_piece<E.lb>(E.la < 6 ? wA + (size_t)E.la * 64 * 9216 : wB + (E.la - 6) * 96, vo[E.lb], wdst + ((E.la + 3) & 3) * UNIT);
    if constexpr (E.lk == L_R) dma_piece<E.lb>(w2src + E.la * UNIT, voff, wdst + E.la * UNIT);
    if constexpr (E.lk == L_CC)
      asm volatile("s_mov_b32 m0, %2\n\ts_mov_b32 exec_hi, 0x3fffff\n\tglobal_load_lds_dwordx4 %0, %1\n\ts_mov_b32 exec_hi, -1"
                   :: "v"(voff), "s"(csrc), "s"(cdst) : "memory");
    if constexpr (E.lk == L_POLL) POLL();
    SB0();
  });
  MFMA_DRAIN();
  epi(IC<8>{});
}

constexpr int K4_LDS = 4 * UNIT + 16384;
constexpr int K4_N = 162, K4_D = 6;
struct K4E { int u, q, hb, b, off, lk, la, lb; };
struct K4T { K4E e[K4_N]; };
constexpr K4T k4_table() {
  K4T t{};
  for (int i = 0; i < K4_N; ++i) {
    const int u = i / 18, r = i % 18;
    K4E e{};
    e.u = u;
    e.q = u < 6 ? r % 9 : (u - 6) * 3 + r / 6;
    e.hb = u < 6 ? 2 * u + r / 9 : 12 + r % 6;
    e.b = e.q == 6 ? 2 : (e.q < 6 ? e.q & 1 : e.q - 7);
    const int pb = e.q == 6 ? 6 : e.q - e.b;
    e.off = u < 6 ? ((r / 9) * 18 + pb) * 1024 : ((u == 8 ? pb - 6 : pb) * 12 + 2 * (r % 6)) * 1024;
    if (u == 0) {
      if (r < 5) { e.lk = L_W; e.la = 2; e.lb = r; }
      else if (r < 9) { e.lk = L_X; e.la = 8 + r - 5; }
      else if (r < 14) { e.lk = L_W; e.la = 3; e.lb = r - 9; }
      else { e.lk = L_X; e.la = 12 + r - 14; }
    } else if (u <= 2) {
      if (r <= 8 && !(r & 1)) { e.lk = L_W; e.la = u + 3; e.lb = r / 2; }
      else if (r >= 10 && !(r & 1)) { e.lk = L_X; e.la = 12 + 4 * u + (r - 10) / 2; }
    } else if (u <= 5) {
      if (r <= 8 && !(r & 1)) { e.lk = L_W; e.la = u + 3; e.lb = r / 2; }
      else if (u < 5 && r >= 10 && r < 16) { e.lk = L_X; e.la = 24 + 6 * (u - 3) + r - 10; }
      else if (u == 5 && r >= 10 && r < 17) { e.lk = L_R; e.la = r - 10; }
    }
    t.e[i] = e;
  }
  return t;
}
constexpr K4T K4TAB = k4_table();
constexpr int k4_send(int i) { return i == 122 ? 0 : i == 134 ? 1 : i == 146 ? 2 : -1; }
constexpr int k4_younger(int J, int lk, int la, int lb, int pre) {
  int n = pre;
  for (int j = 0; j < J; ++j) {
    const K4E& e = K4TAB.e[j];
    if (k4_send(j) >= 0) ++n;
    if (e.lk) ++n;
    if (e.lk == lk && e.la == la && e.lb == lb) n = 0;
  }
  return n;
}
constexpr int k4_xoff(int j) { return j < 2 ? UNIT + j * 16384 : j == 2 ? 4 * UNIT : 2 * UNIT + (j - 3) * 16384; }
__device__ __forceinline__ f32x4 add4(f32x4 a, f32x4 b) {
  f32x4 r;
#pragma unroll
  for (int i = 0; i < 4; ++i) asm("v_add_f32 %0, %1, %2" : "=v"(r[i]) : "v"(a[i]), "v"(b[i]));
  return r;
}

__global__ __launch_bounds__(512, 2) void k34_mlp(const u16* __restrict__ Wg, const u16* __restrict__ Wu, char* __restrict__ ws,
                                                  u16* __restrict__ out) {
  extern __shared__ __attribute__((aligned(16))) char sm[];
  int wave = __builtin_amdgcn_readfirstlane(threadIdx.x >> 6), bid = blockIdx.x;
  k3_gemm1(Wg, Wu, ws, sm);
  int lane;
  asm volatile("v_mbcnt_lo_u32_b32 %0, -1, 0\n\tv_mbcnt_hi_u32_b32 %0, -1, %0\n\ts_waitcnt vmcnt(0)" : "=v"(lane), "+s"(wave), "+s"(bid), "+s"(ws) :: "memory");
  const int tid = wave * 64 + lane;
  GLBP unsigned* grp = (GLBP unsigned*)(ws + OFF_FLG + ((bid >> 6) * 8 + (bid & 7)) * 512);
  XBAR();
  const int wm = wave & 3, wk = wave >> 2;
  const int xcd = bid & 7, idx = bid >> 3;
  const int kb = xcd & 3;
  const int m128 = (xcd >> 2) * 8 + (idx >> 2), nt = idx & 3;
  const unsigned voff = lane * 16;
  const unsigned lds0 = (unsigned)(uintptr_t)(LDSP char*)sm;
  const char* wsrc = ws + OFF_W2 + (size_t)(kb * 4 + nt) * 324 * 1024 + wave * 4608;
  const unsigned wdst = lds0 + wave * 4608;
  const char* h0 = ws + OFF_H2 + ((size_t)(m128 * 4 + wm) * 288 + (kb * 36 + wk) * 2) * 1024;
  u32x4 H[36];
#define LDH(i) ld_frag<((i) & 1) * 1024, H_SC1>(H[i], h0 + ((i) >> 1) * 4096, voff)
  if (wave == 0) {
    unsigned f = 0;
    if (lane == 0) f = __hip_atomic_fetch_add(grp, 1u, __ATOMIC_RELAXED, __HIP_MEMORY_SCOPE_AGENT) + 1u;
    for (unsigned spins = 0; (unsigned)__builtin_amdgcn_readfirstlane(f) < 8u && spins < (1u << 22); ++spins) {
      __builtin_amdgcn_s_sleep(PSL);
      f = __hip_atomic_load(grp, __ATOMIC_RELAXED, __HIP_MEMORY_SCOPE_AGENT);
    }
  }
  XBAR();
  LDH(0); LDH(1); LDH(2); LDH(3);
  LDH(4); LDH(5); LDH(6); LDH(7);
  f32x4 acc[18];
  u32x4 A[K4_D];
  lptr bu[3];
  LDSP char* xw = (LDSP char*)sm + wave * 2048 + lane * 16;
  lptr xr_ = (lptr)sm + (wave ^ 4) * 2048 + lane * 16;
  const int tile = m128 * 4 + nt;
  __amdgpu_buffer_rsrc_t prs = __builtin_amdgcn_make_buffer_rsrc((void*)(ws + OFF_PART + (size_t)tile * PART_TILE), 0, PART_TILE, 0x00020000);
  f32x4 oth[2], f8;
  const int r0 = m128 * 128 + wm * 32, dA = nt * 9 + 2 * kb + wk, d8 = nt * 9 + 8;
  const char* xfA = ws + OFF_XF + (size_t)(r0 >> 5) * 36864 + dA * 1024;
  const char* xf8 = ws + OFF_XF + (size_t)(r0 >> 5) * 36864 + d8 * 1024 + wk * 8;
  const char* gbp = ws + OFF_MOD + 5 * 576 * 4;
  const char* stp = ws + OFF_ST2 + r0 * 4;
  u32x2 xres[3];
  u32x4 gbv[2];
  unsigned stw[2];
  auto rd = [&](auto jc) {
    constexpr int J = decltype(jc)::value;
    constexpr K4E E = K4TAB.e[J];
    if constexpr (J % 18 == 0 && E.u != 7) {
      constexpr int ST = E.u < 6 ? 1024 : 12288;
      bu[2] = (lptr)sm + (E.u & 3) * UNIT + wk * (E.u < 6 ? 9216 : 1024) + lane * 16;
      bu[1] = bu[2] + wk * ST; bu[0] = bu[2] + (1 - wk) * ST;
      asm volatile("" : "+v"(bu[0]), "+v"(bu[1]), "+v"(bu[2]));
    }
    return *(LDSP const u32x4*)(bu[E.b] + E.off);
  };
  WAIT_VM(4);
  XBAR();
  static_for<0, K4_D>([&](auto jc) { A[decltype(jc)::value] = rd(jc); });
  SB0();
  static_for<0, K4_N>([&](auto jc) {
    constexpr int J = decltype(jc)::value;
    constexpr K4E E = K4TAB.e[J];
    constexpr int R = J - 108;
    if constexpr (J % 18 == 12 && J < 108) {
      wait_vm<k4_younger(J, L_W, E.u + 1, 4, 4)>();
      asm volatile("s_waitcnt lgkmcnt(0)" ::: "memory");
      XBAR();
      TS_STEP(E.u + 2)
    }
    if constexpr (R == 3) { wait_vm<k4_younger(J, L_W, 7, 4, 0)>(); XBAR(); }
    if constexpr (R == 7 || R == 19 || R == 31 || R == 49) {
      constexpr int j = R == 49 ? 4 : R / 12, q = E.q - 1;
      *(LDSP f32x4*)(xw + k4_xoff(j)) = acc[2 * q];
      *(LDSP f32x4*)(xw + k4_xoff(j) + 1024) = acc[2 * q + 1];
      SB0();
    }
    if constexpr (R == 43) {
      if (wk) { *(LDSP f32x4*)(xw + k4_xoff(3)) = acc[12]; f8 = acc[13]; } else { *(LDSP f32x4*)(xw + k4_xoff(3)) = acc[13]; f8 = acc[12]; }
      SB0();
    }
    if constexpr (R == 9 || R == 21 || R == 33 || R == 45 || R == 51) {
      if constexpr (R == 21) wait_vm<k4_younger(J, L_W, 8, 4, 0)>();
      asm volatile("s_waitcnt lgkmcnt(%0)" :: "n"(R < 48 ? 2 : 0) : "memory");
      XBAR();
      if constexpr (R == 9 || R == 33) { TS_STEP(R == 9 ? 8 : 9) }
      constexpr int j = R == 51 ? 4 : R / 12;
      oth[0] = *(LDSP const f32x4*)(xr_ + k4_xoff(j));
      if constexpr (j != 3) oth[1] = *(LDSP const f32x4*)(xr_ + k4_xoff(j) + 1024);
      SB0();
    }
    if constexpr (k4_send(J) >= 0) {
      constexpr int j = k4_send(J);
      const f32x4 s0 = add4(acc[4 * j + 2], oth[0]), s1 = add4(acc[4 * j + 3], oth[1]);
      __builtin_amdgcn_raw_buffer_store_b128((u32x4){pk2(s0[0], s0[1]), pk2(s0[2], s0[3]), pk2(s1[0], s1[1]), pk2(s1[2], s1[3])}, prs, voff,
                                             ((((kb + j + 1) & 3) * 4 + kb) * 8 + wave) * 1024, 16);
      SB0();
    }
    if constexpr (R == 47) {
      f8 = add4(f8, oth[0]);
      if (wm != kb)
        __builtin_amdgcn_raw_buffer_store_b64((u32x2){pk2(f8[0], f8[1]), pk2(f8[2], f8[3])}, prs, lane * 8, 131072 + ((wm * 4 + kb) * 2 + wk) * 512, 16);
      SB0();
    }
    constexpr bool first = E.u < 6 ? E.q == 0 : J < 114;
    if constexpr (first) {
      constexpr int hb = E.hb;
      if constexpr (hb >= 2) wait_vm<k4_younger(J, L_X, 2 * hb + 1, 0, hb == 2 ? 2 : 0)>();
      asm volatile("" : "+v"(H[2 * hb]), "+v"(H[2 * hb + 1]));
      const u32x4 l0 = H[2 * hb], l1 = H[2 * hb + 1];
      H[2 * hb] = (u32x4){l0[0], l0[1], l1[0], l1[1]};
      H[2 * hb + 1] = (u32x4){l0[2], l0[3], l1[2], l1[3]};
      asm volatile("s_nop 1" : "+v"(H[2 * hb]), "+v"(H[2 * hb + 1]));
      SB0();
    }
    const u32x4 a = A[J % K4_D];
    mma16t<(J < 9)>(acc[2 * E.q], a, H[2 * E.hb]);
    if constexpr (J + K4_D < K4_N) A[J % K4_D] = rd(IC<J + K4_D>{});
    SB0();
    mma16t<(J < 9)>(acc[2 * E.q + 1], a, H[2 * E.hb + 1]);
    if constexpr (E.lk == L_X) LDH(E.la);
    if constexpr (E.lk == L_W) dma_piece<E.lb>(wsrc + E.la * UNIT, voff, wdst + (E.la & 3) * UNIT);
    if constexpr (E.lk == L_R) {
      if constexpr (E.la < 3)
        asm volatile("global_load_dwordx2 %0, %1, %2" : "=v"(xres[E.la]) : "v"(voff), "s"(E.la == 2 ? xf8 : xfA + E.la * 8) : "memory");
      else if constexpr (E.la < 5)
        asm volatile("global_load_dwordx4 %0, %1, %2" : "=v"(gbv[E.la - 3]) : "v"(lane & 48), "s"(gbp + (E.la == 3 ? dA : d8) * 64) : "memory");
      else
        asm volatile("global_load_dword %0, %1, %2 sc1" : "=v"(stw[E.la - 5]) : "v"((lane & 15) * 4), "s"(stp + (E.la - 5) * 64) : "memory");
    }
    SB0();
  });
  WAIT_VM(0);
  asm volatile("" : "+v"(xres[0]), "+v"(xres[1]), "+v"(xres[2]), "+v"(gbv[0]), "+v"(gbv[1]), "+v"(stw[0]), "+v"(stw[1]));
  XBAR();
  GLBP unsigned* cnt = (GLBP unsigned*)(ws + OFF_CNT + tile * 128);
  if (tid == 0) __hip_atomic_fetch_add(cnt, 1u, __ATOMIC_RELAXED, __HIP_MEMORY_SCOPE_AGENT);
  MFMA_DRAIN();
  f32x4 fin[3] = {add4(acc[16], oth[0]), add4(acc[17], oth[1]), f8};
  float rs2[3];
#pragma unroll
  for (int i = 0; i < 2; ++i) { const float r = 1.f / hi16(stw[i]); rs2[i] = r * r; }
  rs2[2] = wk ? rs2[1] : rs2[0];
  if (wave == 0) {
    for (unsigned spins = 0; spins < (1u << 22); ++spins) {
      if ((unsigned)__builtin_amdgcn_readfirstlane(__hip_atomic_load(cnt, __ATOMIC_RELAXED, __HIP_MEMORY_SCOPE_AGENT)) >= 4u) break;
      __builtin_amdgcn_s_sleep(1);
    }
  }
  BARRIER();
  asm volatile(".Lk34_tail:");
  u32x4 pp[3];
  u32x2 p8[3];
#pragma unroll
  for (int j = 0; j < 3; ++j) {
    const int src = (kb + 1 + j) & 3;
    pp[j] = __builtin_amdgcn_raw_buffer_load_b128(prs, voff, ((kb * 4 + src) * 8 + wave) * 1024, 16);
    p8[j] = __builtin_amdgcn_raw_buffer_load_b64(prs, lane * 8, 131072 + ((kb * 4 + src) * 2 + wk) * 512, 16);
  }
#pragma unroll
  for (int i = 0; i < 3; ++i) {
    if (i == 2 && wm != kb) break;
    u32x2 o;
#pragma unroll
    for (int e = 0; e < 2; ++e) {
      const unsigned q0 = i < 2 ? pp[0][2 * i + e] : p8[0][e], q1 = i < 2 ? pp[1][2 * i + e] : p8[1][e], q2 = i < 2 ? pp[2][2 * i + e] : p8[2][e];
      float a = ((fin[i][2 * e] + lo16(q0)) + (lo16(q1) + lo16(q2))) * rs2[i];
      float b = ((fin[i][2 * e + 1] + hi16(q0)) + (hi16(q1) + hi16(q2))) * rs2[i];
      bfr2(a, b);
      const unsigned g0 = gbv[i >> 1][2 * e], g1 = gbv[i >> 1][2 * e + 1];
      a *= __uint_as_float(g0); b *= __uint_as_float(g1); bfr2(a, b);
      o[e] = pk2(lo16(xres[i][e]) + a, hi16(xres[i][e]) + b);
    }
    const int m = r0 + (i < 2 ? i : wk) * 16 + (lane & 15);
    *(u32x2*)(out + (size_t)m * D_ + (i < 2 ? dA : d8) * 16 + (lane >> 4) * 4) = o;
  }
}

extern "C" void kernel_launch(void* const* d_in, const int* in_sizes, int n_in, void* d_out, int out_size,
                              void* d_ws, size_t ws_size, hipStream_t stream) {
  const u16* A_q = (const u16*)d_in[0];
  const u16* A_k = (const u16*)d_in[1];
  const u16* A_v = (const u16*)d_in[2];
  const u16* A_sh = (const u16*)d_in[3];
  const u16* A_sk = (const u16*)d_in[4];
  const u16* A_D = (const u16*)d_in[5];
  const u16* Wg = (const u16*)d_in[7];
  const u16* Wu = (const u16*)d_in[8];
  const u16* Wd = (const u16*)d_in[9];
  const u16* cv = (const u16*)d_in[10];
  const u16* wm = (const u16*)d_in[11];
  char* ws = (char*)d_ws;
  (void)hipFuncSetAttribute((const void*)k1_conv, hipFuncAttributeMaxDynamicSharedMemorySize, K1_LDS);
  (void)hipFuncSetAttribute((const void*)k34_mlp, hipFuncAttributeMaxDynamicSharedMemorySize, K4_LDS);
  k0_prep<<<K0_GRID, 256, 0, stream>>>(A_q, A_k, A_v, A_sh, cv, wm, A_sk, ws);
  k1_conv<<<K1_CONV + K1_W, 512, K1_LDS, stream>>>(A_q, A_sk, A_D, Wg, Wu, Wd, ws);
  k34_mlp<<<256, 512, K4_LDS, stream>>>(Wg, Wu, ws, (u16*)d_out);
}
```

```cpp
#include <hip/hip_runtime.h>
#include <hip/hip_bf16.h>
#include <stdint.h>

typedef unsigned short u16;
typedef __attribute__((ext_vector_type(2))) __bf16 bf16x2;
typedef __attribute__((ext_vector_type(8))) __bf16 bf16x8;
typedef __attribute__((ext_vector_type(2))) float f32x2;
typedef __attribute__((ext_vector_type(4))) float f32x4;
typedef __attribute__((ext_vector_type(16))) float f32x16;
typedef __attribute__((ext_vector_type(4))) unsigned u32x4;
typedef __attribute__((ext_vector_type(2))) unsigned u32x2;
typedef __attribute__((ext_vector_type(4))) short s16x4;
#define LDSP __attribute__((address_space(3)))
#define GLBP __attribute__((address_space(1)))

constexpr int S_ = 2048, D_ = 576, F_ = 4608;

constexpr size_t OFF_CNT = 0;
constexpr size_t OFF_MOD = 8192;
constexpr size_t OFF_ST1 = OFF_MOD + 16384;
constexpr size_t OFF_ST2 = OFF_ST1 + 8192;
constexpr size_t OFF_CC  = OFF_ST2 + 8192;
constexpr size_t OFF_UT  = OFF_CC + 32 * 6912;
constexpr size_t OFF_FLG = OFF_UT + (size_t)D_ * S_ * 2;
constexpr size_t OFF_XF  = OFF_FLG + 16384;
constexpr size_t OFF_XS  = OFF_XF + (size_t)D_ * S_ * 2;
constexpr size_t OFF_W2  = OFF_XS + (size_t)D_ * S_ * 2;
constexpr size_t OFF_H2  = OFF_W2 + (size_t)D_ * F_ * 2;
constexpr size_t OFF_PART = OFF_H2 + (size_t)S_ * F_ * 2;
constexpr int PART_TILE = 131072 + 16384;

__device__ __forceinline__ unsigned pk2(float a, float b) {
  f32x2 v = {a, b};
  return __builtin_bit_cast(unsigned, __builtin_convertvector(v, bf16x2));
}
__device__ __forceinline__ float lo16(unsigned u) { return __uint_as_float(u << 16); }
__device__ __forceinline__ float hi16(unsigned u) { return __uint_as_float(u & 0xffff0000u); }
__device__ __forceinline__ float bfr(float a) { return lo16(pk2(a, a)); }
__device__ __forceinline__ void bfr2(float& a, float& b) { unsigned u = pk2(a, b); a = lo16(u); b = hi16(u); }
__device__ __forceinline__ float bf2f(u16 x) { return __uint_as_float(((unsigned)x) << 16); }
__device__ __forceinline__ s16x4 tr_read(const char* p) {
  return __builtin_amdgcn_ds_read_tr16_b64_v4i16((LDSP s16x4*)p);
}
__device__ __forceinline__ u32x4 tr_read2(const char* p0, const char* p1) {
  s16x4 a = tr_read(p0), b = tr_read(p1);
  u32x2 x = __builtin_bit_cast(u32x2, a), y = __builtin_bit_cast(u32x2, b);
  u32x4 r = {x[0], x[1], y[0], y[1]};
  return r;
}

#define BARRIER() { asm volatile("s_waitcnt lgkmcnt(0)" ::: "memory"); __builtin_amdgcn_s_barrier(); asm volatile("" ::: "memory"); }
#define PIN4(a, b, c, d) asm volatile("" : "+v"(a), "+v"(b), "+v"(c), "+v"(d))
typedef LDSP const char* lptr;
__device__ __forceinline__ u32x4 tr_read2l(lptr p0, lptr p1) {
  s16x4 a = __builtin_amdgcn_ds_read_tr16_b64_v4i16((LDSP s16x4*)p0), b = __builtin_amdgcn_ds_read_tr16_b64_v4i16((LDSP s16x4*)p1);
  u32x2 x = __builtin_bit_cast(u32x2, a), y = __builtin_bit_cast(u32x2, b);
  u32x4 r = {x[0], x[1], y[0], y[1]};
  return r;
}

#define CODE_TAIL 1900
#define NT 15
template <int B> __device__ __forceinline__ u32x4 ldv(const void* p) {
  if constexpr ((NT >> B) & 1) return __builtin_nontemporal_load((const u32x4*)p); else return *(const u32x4*)p;
}
#define W2_AUX 16
__device__ __forceinline__ void wd_load(u32x4 (&wd)[9], const u16* Wd, int pc, int lane) {
#pragma unroll
  for (int i = 0; i < 9; ++i) {
    const int c = i * 64 + lane;
    wd[i] = ldv<3>((const char*)Wd + (size_t)(pc >> 2) * 36864 + (pc & 3) * 288 + (c / 18) * 1152 + (c % 18) * 16);
  }
}
__device__ __forceinline__ void wd_store(const u32x4 (&wd)[9], char* im, char* ws, int pc, int lane) {
#pragma unroll
  for (int i = 0; i < 9; ++i) *(u32x4*)(im + i * 1024 + lane * 16) = wd[i];
  const char* base = im + ((lane >> 4) * 4 + ((lane & 15) >> 2)) * 288 + (lane & 3) * 8;
  const int kb = (pc >> 2) / 36, Tl = (pc >> 2) % 36;
  __amdgpu_buffer_rsrc_t wrs = __builtin_amdgcn_make_buffer_rsrc((void*)(ws + OFF_W2 + (size_t)(kb * 4 + (pc & 3)) * 331776), 0, 331776, 0x00020000);
  asm volatile("s_waitcnt lgkmcnt(0)" ::: "memory");
#pragma unroll
  for (int i = 0; i < 9; ++i) {
    const u32x4 r = tr_read2(base + i * 32, base + i * 32 + 16 * 288);
    const int o = ((i >> 1) - kb) & 3;
    const int pos = i == 8 ? 6 : (o ? 2 * (o - 1) + (i & 1) : 7 + (i & 1));
    __builtin_amdgcn_raw_buffer_store_b128(r, wrs, lane * 16, (Tl < 24 ? Tl * 9 + pos : 216 + pos * 12 + Tl - 24) * 1024, W2_AUX);
  }
  __builtin_amdgcn_sched_barrier(0);
  asm volatile("s_nop 1" ::: "memory");
}
#define K0_AUX 16
constexpr int K0_MOD = 108, K0_KV = 288, K0_ST = 64;
constexpr int K0_GRID = K0_MOD + K0_KV + K0_ST;

__global__ __launch_bounds__(256) void k0_prep(const u16* __restrict__ xq, const u16* __restrict__ kk,
                                               const u16* __restrict__ vv, const u16* __restrict__ shift_k,
                                               const u16* __restrict__ cvec, const u16* __restrict__ w_mod,
                                               const u16* __restrict__ ssm_k, char* __restrict__ ws) {
  __shared__ __attribute__((aligned(16))) char sm[8192];
  const int tid = threadIdx.x, lane = tid & 63, wave = tid >> 6;
  int bid = blockIdx.x;
  if (bid < K0_MOD) {
    if (bid == 0 && tid < 64) *(unsigned*)(ws + OFF_CNT + tid * 128) = 0u;
    if (bid == 9) *(unsigned*)(ws + OFF_FLG + tid * 64) = 0u;
    if (bid >= 1 && bid <= 8) ((unsigned*)(ws + OFF_ST2))[(bid - 1) * 256 + tid] = 0u;
    const int c4 = tid & 3, rg = tid >> 2;
    u32x4 w[9];
    float sc[9];
#pragma unroll
    for (int i = 0; i < 9; ++i) {
      const int row = rg + 64 * i;
      w[i] = ldv<0>(w_mod + (size_t)row * 3456 + bid * 32 + c4 * 8);
      sc[i] = bf2f(cvec[row]);
    }
    float acc[8];
#pragma unroll
    for (int e = 0; e < 8; ++e) acc[e] = 0.f;
#pragma unroll
    for (int i = 0; i < 9; ++i) {
      const float s = bfr(sc[i] / (1.f + __expf(-sc[i])));
#pragma unroll
      for (int e = 0; e < 4; ++e) {
        acc[2 * e] += s * lo16(w[i][e]);
        acc[2 * e + 1] += s * hi16(w[i][e]);
      }
    }
#pragma unroll
    for (int e = 0; e < 8; ++e) {
#pragma unroll
      for (int o = 4; o < 64; o <<= 1) acc[e] += __shfl_xor(acc[e], o);
    }
    float* red = (float*)sm;
    if (lane < 4) {
#pragma unroll
      for (int e = 0; e < 8; ++e) red[wave * 32 + lane * 8 + e] = acc[e];
    }
    __syncthreads();
    if (tid < 32) {
      const float r = (red[tid] + red[32 + tid]) + (red[64 + tid] + red[96 + tid]);
      ((float*)(ws + OFF_MOD))[bid * 32 + tid] = bfr(r);
    }
    return;
  }
  bid -= K0_MOD;
  if (bid < K0_KV) {
    const int cb = bid % 9, tb = bid / 9;
    const int c8 = tid & 7, rp = tid >> 3;
    const int r0 = tb * 64 + rp * 2;
    const int ch0 = cb * 64 + c8 * 8;
    u32x4 kr[5], vr[2], sw[4];
    unsigned warm = 0;
    {
      const char* pc;
      unsigned len;
      asm volatile("s_getpc_b64 %0\n.Lk0_here:\n\ts_mov_b32 %1, .Lk34_tail-.Lk0_here" : "=s"(pc), "=s"(len));
      const unsigned off = (bid >> 3) * 8192 + (tid & 127) * 64;
      const char* wp = tid < 128 ? (const char*)ssm_k + (blockIdx.x & 7) * 294912 + off : pc + off;
      if (tid < 128 || off < len + CODE_TAIL) asm volatile("global_load_dword %0, %1, off" : "+v"(warm) : "v"(wp) : "memory");
    }
#pragma unroll
    for (int i = 0; i < 5; ++i) {
      const int r = r0 - 3 + i;
      const int rc = r < 0 ? 0 : r;
      kr[i] = ldv<1>(kk + (size_t)rc * D_ + ch0);
      if (r < 0) kr[i] = (u32x4){0u, 0u, 0u, 0u};
    }
#pragma unroll
    for (int i = 0; i < 2; ++i) vr[i] = ldv<2>(vv + (size_t)(r0 + i) * D_ + ch0);
#pragma unroll
    for (int i = 0; i < 4; ++i) sw[i] = *(const u32x4*)(shift_k + (size_t)ch0 * 4 + i * 8);
    char* tu = sm;
    const int q = rp >> 2, inner = rp & 3;
#pragma unroll
    for (int e = 0; e < 8; ++e) {
      const unsigned wa = sw[e >> 1][(e & 1) * 2], wb = sw[e >> 1][(e & 1) * 2 + 1];
      const float w0 = lo16(wa), w1 = hi16(wa), w2 = lo16(wb), w3 = hi16(wb);
      float kf[5];
#pragma unroll
      for (int i = 0; i < 5; ++i) kf[i] = (e & 1) ? hi16(kr[i][e >> 1]) : lo16(kr[i][e >> 1]);
      const float v0 = (e & 1) ? hi16(vr[0][e >> 1]) : lo16(vr[0][e >> 1]);
      const float v1 = (e & 1) ? hi16(vr[1][e >> 1]) : lo16(vr[1][e >> 1]);
      float a0 = kf[0] * w3, a1 = kf[1] * w3; bfr2(a0, a1);
      float t0 = kf[1] * w2, t1 = kf[2] * w2; bfr2(t0, t1);
      a0 += t0; a1 += t1; bfr2(a0, a1);
      t0 = kf[2] * w1; t1 = kf[3] * w1; bfr2(t0, t1);
      a0 += t0; a1 += t1; bfr2(a0, a1);
      t0 = kf[3] * w0; t1 = kf[4] * w0; bfr2(t0, t1);
      a0 += t0; a1 += t1; bfr2(a0, a1);
      const unsigned kvp = pk2(a0 * v0, a1 * v1);
      const int off = (c8 * 8 + e) * 128 + ((q ^ c8) * 16) + inner * 4;
      *(unsigned*)(tu + off) = kvp;
    }
    __syncthreads();
    asm volatile("s_waitcnt vmcnt(0)" : "+v"(warm));
    __amdgpu_buffer_rsrc_t urs = __builtin_amdgcn_make_buffer_rsrc((void*)(ws + OFF_UT), 0, D_ * S_ * 2, 0x00020000);
#pragma unroll
    for (int i = 0; i < 2; ++i) {
      const int p = tid + 256 * i, ch = p >> 3, qq = p & 7;
      const int off = ch * 128 + ((qq ^ (ch >> 3)) * 16);
      __builtin_amdgcn_raw_buffer_store_b128(*(const u32x4*)(tu + off), urs, ((cb * 64 + ch) * S_ + tb * 64 + qq * 8) * 2, 0, K0_AUX);
    }
    return;
  }
  bid -= K0_KV;
  {
    const int row = bid * 32 + wave * 8 + (lane >> 3), c = lane & 7;
    u32x4 xr[9];
#pragma unroll
    for (int i = 0; i < 9; ++i) xr[i] = ldv<5>(xq + (size_t)row * D_ + (c + 8 * i) * 8);
    float s = 0.f;
#pragma unroll
    for (int i = 0; i < 9; ++i)
#pragma unroll
      for (int e = 0; e < 4; ++e) s += lo16(xr[i][e]) + hi16(xr[i][e]);
    s += __shfl_xor(s, 1); s += __shfl_xor(s, 2); s += __shfl_xor(s, 4);
    const float mean = s * (1.f / 576.f);
    float ss = 0.f;
#pragma unroll
    for (int i = 0; i < 9; ++i)
#pragma unroll
      for (int e = 0; e < 4; ++e) {
        const float a = lo16(xr[i][e]) - mean, b = hi16(xr[i][e]) - mean;
        ss += a * a + b * b;
      }
    ss += __shfl_xor(ss, 1); ss += __shfl_xor(ss, 2); ss += __shfl_xor(ss, 4);
    const float var = bfr(ss * (1.f / 576.f));
    const float sd = sqrtf(bfr(var + 1e-5f));
    if (c == 0) ((unsigned*)(ws + OFF_ST1))[row] = pk2(mean, sd);
  }
}

constexpr int K1_CONV = 144, K1_W = 108;
constexpr int KC_STRIDE = 4416;
constexpr int CH_BYTES = 4 * KC_STRIDE + 4672;
constexpr int K1_XCH = 4 * CH_BYTES;
constexpr int K1_X1 = K1_XCH + 32768;
constexpr int K1_LDS = K1_X1 + 32768;

#define K1_AUX 16
__global__ __launch_bounds__(512, 2) void k1_conv(const u16* __restrict__ xq, const u16* __restrict__ ssm_k, const u16* __restrict__ ssm_D,
                                                  const u16* __restrict__ Wg, const u16* __restrict__ Wu,
                                                  const u16* __restrict__ Wd, char* __restrict__ ws) {
  extern __shared__ __attribute__((aligned(16))) char sm[];
  const int tid = threadIdx.x, lane = tid & 63, wave = __builtin_amdgcn_readfirstlane(tid >> 6);
  const float* mod = (const float*)(ws + OFF_MOD);
  int bid = blockIdx.x;
  if (bid < K1_CONV) {
    const int cg = (bid & 7) * 18 + (bid >> 3);
    const int cl = wave >> 1, w2 = wave & 1;
    const int d = cg * 4 + cl;
    char* chb = sm + cl * CH_BYTES;
    char* ub = chb + 4 * KC_STRIDE;
    const int t128 = tid & 127;
    const char* krow = (const char*)ssm_k + (size_t)d * 4096;
    const char* urow = ws + OFF_UT + (size_t)d * 4096;
    u32x4 ka[2], ua[2];
    u32x2 kb[2];
#pragma unroll
    for (int i = 0; i < 2; ++i) {
      const int c = t128 + 128 * i;
      ka[i] = *(const u32x4*)(krow + c * 16);
      const int cn = c < 255 ? c + 1 : 255;
      kb[i] = *(const u32x2*)(krow + cn * 16);
      if (c == 255) kb[i] = (u32x2){0u, 0u};
      ua[i] = *(const u32x4*)(urow + c * 16);
    }
    const int li = lane & 15, g = lane >> 4;
    const float Dd = bf2f(ssm_D[d]);
    {
      const u32x4 z = {0u, 0u, 0u, 0u};
      if (t128 < 32) *(u32x4*)(ub + t128 * 16) = z;
      else if (t128 < 32 + 12) {
        const int i = t128 - 32;
        *(u32x4*)(chb + (i / 3) * KC_STRIDE + (i % 3) * 16) = z;
      } else if (t128 < 32 + 12 + 16) {
        const int i = t128 - 44;
        *(u32x4*)(chb + (i >> 2) * KC_STRIDE + 64 + 4096 + (i & 3) * 16) = z;
      } else if (t128 < 64) {
        *(u32x4*)(ub + 4608 + (t128 - 60) * 16) = z;
      }
    }
#pragma unroll
    for (int i = 0; i < 2; ++i) {
      const int c = t128 + 128 * i;
      const unsigned w0 = ka[i][0], w1 = ka[i][1], w2_ = ka[i][2], w3 = ka[i][3], w4 = kb[i][0], w5 = kb[i][1];
      const unsigned a0 = __builtin_amdgcn_alignbit(w1, w0, 16), a1 = __builtin_amdgcn_alignbit(w2_, w1, 16),
                     a2 = __builtin_amdgcn_alignbit(w3, w2_, 16), a3 = __builtin_amdgcn_alignbit(w4, w3, 16),
                     a4 = __builtin_amdgcn_alignbit(w5, w4, 16);
      char* p = chb + 64 + c * 16;
      *(u32x4*)(p) = ka[i];
      *(u32x4*)(p + KC_STRIDE) = (u32x4){a0, a1, a2, a3};
      *(u32x4*)(p + 2 * KC_STRIDE) = (u32x4){w1, w2_, w3, w4};
      *(u32x4*)(p + 3 * KC_STRIDE) = (u32x4){a1, a2, a3, a4};
      *(u32x4*)(ub + 512 + c * 16) = ua[i];
      if (c == 0) {
        *(u32x4*)(p - 16) = (u32x4){0u, 0u, 0u, 0u};
        *(u32x4*)(p - 16 + KC_STRIDE) = (u32x4){0u, 0u, 0u, w0 << 16};
        *(u32x4*)(p - 16 + 2 * KC_STRIDE) = (u32x4){0u, 0u, 0u, w0};
        *(u32x4*)(p - 16 + 3 * KC_STRIDE) = (u32x4){0u, 0u, w0 << 16, a0};
      }
    }
    u32x2 xv[4];
    unsigned st[4];
#pragma unroll
    for (int i = 0; i < 4; ++i) {
      const int s = (wave * 8 + (i >> 1) * 4 + g) * 32 + (i & 1) * 16 + li;
      st[i] = ((const unsigned*)(ws + OFF_ST1))[s];
    }
    f32x4 sa = *(const f32x4*)(mod + cg * 4), ca = *(const f32x4*)(mod + 576 + cg * 4);
    f32x4 ga = *(const f32x4*)(mod + 1152 + cg * 4), cb = *(const f32x4*)(mod + 2304 + cg * 4);
    BARRIER();
    const int q = li >> 2, p4 = li & 3;
    const char* abase = chb + ((4 - q) & 3) * KC_STRIDE + 2 * (32 - 8 * g - (q ? 4 : 0) + 4 * p4) - 8;
    const char* bbase = ub + 2 * (256 + 16 * li + 8 * g);
    f32x4 acc[8];
#pragma unroll
    for (int b = 0; b < 8; ++b) acc[b] = (f32x4){0.f, 0.f, 0.f, 0.f};
    u32x2 hp[4], tp[4];
    auto hfun = [&](int i) {
      asm volatile("" : "+v"(st[i]), "+v"(xv[i]));
      const float mu = lo16(st[i]), rs = __builtin_amdgcn_rcpf(hi16(st[i]));
#pragma unroll
      for (int e = 0; e < 2; ++e) {
        float h0 = lo16(xv[i][e]) - mu, h1 = hi16(xv[i][e]) - mu; bfr2(h0, h1);
        h0 *= rs; h1 *= rs; bfr2(h0, h1);
        h0 *= ca[2 * e]; h1 *= ca[2 * e + 1]; bfr2(h0, h1);
        hp[i][e] = pk2(h0 + sa[2 * e], h1 + sa[2 * e + 1]);
      }
      asm volatile("" : "+v"(hp[i]));
    };
    const char* ap = abase + 64 * (1 + w2);
    const char* bp = bbase - 64 * (1 + w2) - 384;
    bf16x8 A[2][8], B[2][8];
#pragma unroll
    for (int i = 0; i < 8; ++i) {
      A[0][i] = __builtin_bit_cast(bf16x8, tr_read2(ap + 512 * i + 8, ap + 512 * i));
      B[0][i] = *(const bf16x8*)(bp + 384 + 512 * i);
    }
    __builtin_amdgcn_sched_barrier(0);
#pragma unroll
    for (int it = 0; it < 4; ++it) {
#pragma unroll
      for (int b = 0; b < 8; ++b) {
        if (it < 3) {
          A[(it + 1) & 1][b] = __builtin_bit_cast(bf16x8, tr_read2(ap + 128 * (it + 1) + 512 * b + 8, ap + 128 * (it + 1) + 512 * b));
          B[(it + 1) & 1][b] = *(const bf16x8*)(bp + 384 - 128 * (it + 1) + 512 * b);
        }
        if ((it == 0 && (b == 2 || b == 6)) || ((it == 1 || it == 2) && b == 2)) {
          const int i = it ? it + 1 : b == 6;
          xv[i] = *(const u32x2*)(xq + (size_t)((wave * 8 + (i >> 1) * 4 + g) * 32 + (i & 1) * 16 + li) * D_ + cg * 4);
        }
        if (it == 1 && b >= 3 && b < 7) tp[b - 3] = *(const u32x2*)(ub + 512 + (256 * (4 * w2 + b - 3) + 16 * li + 4 * g) * 2);
        if (it == 1 && b >= 4) {
          const u32x2 uu = tp[b - 4];
          tp[b - 4] = (u32x2){pk2(lo16(uu[0]) * Dd, hi16(uu[0]) * Dd), pk2(lo16(uu[1]) * Dd, hi16(uu[1]) * Dd)};
          asm volatile("" : "+v"(tp[b - 4]));
        }
        if (it == 2 && b == 6) {
          asm volatile("" : "+v"(ca), "+v"(cb));
#pragma unroll
          for (int e = 0; e < 4; ++e) { ca[e] = bfr(1.f + ca[e]); cb[e] = bfr(1.f + cb[e]); }
          asm volatile("" : "+v"(ca), "+v"(cb));
        }
        if (it == 2 && b == 7) hfun(0);
        if (it == 3 && b >= 5) hfun(b - 4);
#pragma unroll
        for (int qq = 0; qq <= b; ++qq)
          acc[b] = __builtin_amdgcn_mfma_f32_16x16x32_bf16(A[it & 1][qq], B[it & 1][b - qq], acc[b], 0, 0, 0);
        __builtin_amdgcn_sched_barrier(0);
      }
    }
    char* xch = sm + K1_XCH;
    f32x4 fin[4];
    if (w2 == 0) {
#pragma unroll
      for (int i = 0; i < 4; ++i) { *(f32x4*)(xch + wave * 4096 + i * 1024 + lane * 16) = acc[4 + i]; fin[i] = acc[i]; }
    } else {
#pragma unroll
      for (int i = 0; i < 4; ++i) { *(f32x4*)(xch + wave * 4096 + i * 1024 + lane * 16) = acc[i]; fin[i] = acc[4 + i]; }
    }
    {
      const bf16x8 A0 = __builtin_bit_cast(bf16x8, tr_read2(abase + 8, abase));
#pragma unroll
      for (int i = 0; i < 4; ++i) {
        const bf16x8 Bd = *(const bf16x8*)(bbase + 512 * (4 * w2 + i));
        fin[i] = __builtin_amdgcn_mfma_f32_16x16x32_bf16(A0, Bd, fin[i], 0, 0, 0);
      }
    }
    PIN4(fin[0], fin[1], fin[2], fin[3]);
    __syncthreads();
    char* ys = sm + K1_X1;
#pragma unroll
    for (int i = 0; i < 4; ++i) {
      const f32x4 o = *(const f32x4*)(xch + (wave ^ 1) * 4096 + i * 1024 + lane * 16);
      u32x2 y;
#pragma unroll
      for (int e = 0; e < 2; ++e) {
        float y0 = fin[i][2 * e] + o[2 * e], y1 = fin[i][2 * e + 1] + o[2 * e + 1]; bfr2(y0, y1);
        y[e] = pk2(y0 + lo16(tp[i][e]), y1 + hi16(tp[i][e]));
      }
      *(u32x2*)(ys + cl * 4128 + (256 * (4 * w2 + i) + 16 * li + 4 * g) * 2) = y;
    }
    __syncthreads();
    __amdgpu_buffer_rsrc_t xrs = __builtin_amdgcn_make_buffer_rsrc((void*)(ws + OFF_XF), 0, 2 * D_ * S_ * 2, 0x00020000);
#pragma unroll
    for (int j = 0; j < 2; ++j) {
      const int mt = wave * 8 + j * 4 + g;
      const char* yp = ys + q * 4128 + mt * 64 + p4 * 8;
      const u32x4 y = tr_read2(yp, yp + 32);
      u32x4 o1, o2;
#pragma unroll
      for (int e = 0; e < 4; ++e) {
        const unsigned hh = hp[2 * j + (e >> 1)][e & 1], xx = xv[2 * j + (e >> 1)][e & 1];
        const int c = (e & 1) * 2;
        float y0 = lo16(y[e]) * lo16(hh), y1 = hi16(y[e]) * hi16(hh); bfr2(y0, y1);
        y0 *= ga[c]; y1 *= ga[c + 1]; bfr2(y0, y1);
        const unsigned r = pk2(lo16(xx) + y0, hi16(xx) + y1);
        o1[e] = r;
        o2[e] = pk2(lo16(r) * cb[c], hi16(r) * cb[c + 1]);
      }
      const int off = mt * 36864 + cg * 256 + li * 16;
      __builtin_amdgcn_raw_buffer_store_b128(o2, xrs, off, D_ * S_ * 2, K1_AUX);
      __builtin_amdgcn_raw_buffer_store_b128(o1, xrs, off, 0, K1_AUX);
    }
    return;
  }
  bid -= K1_CONV;
  {
    const int cbk = bid % 36, kt = bid / 36;
    const int mat = cbk / 18, cblk = cbk % 18;
    const u16* W = mat ? Wu : Wg;
    const int p = tid & 31, rg = tid >> 5;
    u32x4 w[12];
    float cs[12], sb[12];
#pragma unroll
    for (int i = 0; i < 12; ++i) {
      const int k = kt * 192 + rg + 16 * i;
      w[i] = ldv<4>(W + (size_t)k * F_ + cblk * 256 + p * 8);
    }
    __builtin_amdgcn_sched_barrier(0);
#pragma unroll
    for (int i = 0; i < 12; ++i) {
      const int k = kt * 192 + rg + 16 * i;
      cs[i] = mod[4 * 576 + k];
      sb[i] = mod[3 * 576 + k];
    }
    const int pc = wave * K1_W + bid;
    u32x4 wd[9];
    if (pc < 576) wd_load(wd, Wd, pc, lane);
    __builtin_amdgcn_sched_barrier(0);
    float a1[8], a2[8];
#pragma unroll
    for (int e = 0; e < 8; ++e) { a1[e] = 0.f; a2[e] = 0.f; }
#pragma unroll
    for (int i = 0; i < 12; ++i) {
      const float c1 = bfr(1.f + cs[i]);
#pragma unroll
      for (int e = 0; e < 4; ++e) {
        const float l = lo16(w[i][e]), h = hi16(w[i][e]);
        a1[2 * e] += c1 * l; a1[2 * e + 1] += c1 * h;
        a2[2 * e] += sb[i] * l; a2[2 * e + 1] += sb[i] * h;
      }
    }
    float* red = (float*)(sm + 110592);
#pragma unroll
    for (int e = 0; e < 8; ++e) { a1[e] += __shfl_xor(a1[e], 32); a2[e] += __shfl_xor(a2[e], 32); }
    if (lane < 32) {
      *(f32x4*)(red + wave * 512 + p * 8) = (f32x4){a1[0], a1[1], a1[2], a1[3]};
      *(f32x4*)(red + wave * 512 + p * 8 + 4) = (f32x4){a1[4], a1[5], a1[6], a1[7]};
    } else {
      *(f32x4*)(red + wave * 512 + 256 + p * 8) = (f32x4){a2[0], a2[1], a2[2], a2[3]};
      *(f32x4*)(red + wave * 512 + 256 + p * 8 + 4) = (f32x4){a2[4], a2[5], a2[6], a2[7]};
    }
    BARRIER();
    {
      float r = 0.f;
#pragma unroll
      for (int i = 0; i < 8; ++i) r += red[i * 512 + tid];
      const int which = tid >> 8, col = tid & 255;
      const int ft = cblk * 16 + (col >> 4);
      ((float*)(ws + OFF_CC))[(ft / 9) * 1728 + kt * 576 + which * 288 + (ft % 9) * 32 + mat * 16 + (col & 15)] = r;
    }
    if (pc < 576) wd_store(wd, sm + wave * 9216, ws, pc, lane);
  }
}

constexpr int UNIT = 36864;

__device__ __forceinline__ void dma_unit(const char* gbase  , unsigned voff, unsigned lds_dst  ) {
  unsigned keep;
  asm volatile(
      "s_mov_b32 %0, m0\n\t"
      "s_mov_b32 m0, %3\n\t"
      "s_mov_b32 exec_hi, 0\n\t"
      "global_load_lds_dwordx4 %1, %2\n\t"
      "s_mov_b32 exec_hi, -1\n\t"
      "global_load_lds_dwordx4 %1, %2 offset:512\n\t"
      "global_load_lds_dwordx4 %1, %2 offset:1536\n\t"
      "global_load_lds_dwordx4 %1, %2 offset:2560\n\t"
      "global_load_lds_dwordx4 %1, %2 offset:3584\n\t"
      "s_mov_b32 m0, %0"
      : "=&s"(keep)
      : "v"(voff), "s"(gbase), "s"(lds_dst)
      : "memory");
}
template <int OFF, bool SC1 = false>
__device__ __forceinline__ void ld_frag(u32x4& dst, const char* gbase  , unsigned voff) {
  if constexpr (SC1) asm volatile("global_load_dwordx4 %0, %1, %2 offset:%3 sc1" : "=v"(dst) : "v"(voff), "s"(gbase), "i"(OFF) : "memory");
  else asm volatile("global_load_dwordx4 %0, %1, %2 offset:%3" : "=v"(dst) : "v"(voff), "s"(gbase), "i"(OFF) : "memory");
}
#define H_SC1 1
#define TS_STEP(i)
#define H2_AUX 16
#define WAIT_VM(n) asm volatile("s_waitcnt vmcnt(" #n ")" ::: "memory")

constexpr int K3_UP = 18432;
constexpr int K3_CCRAW = 4 * UNIT;
constexpr int K3_CC = K3_CCRAW + 6912;
constexpr int K3_LDS = K3_CC + 4608;
constexpr int K3_D = 6;

__device__ __forceinline__ void dma_rows(const char* gbase  , const unsigned (&vo)[5], unsigned lds_dst  ) {
  asm volatile(
      "s_mov_b32 m0, %6\n\t"
      "s_mov_b32 exec_hi, 0\n\t"
      "global_load_lds_dwordx4 %0, %5\n\t"
      "s_mov_b32 exec_hi, -1\n\t"
      "global_load_lds_dwordx4 %1, %5 offset:512\n\t"
      "global_load_lds_dwordx4 %2, %5 offset:1536\n\t"
      "global_load_lds_dwordx4 %3, %5 offset:2560\n\t"
      "global_load_lds_dwordx4 %4, %5 offset:3584"
      :
      : "v"(vo[0]), "v"(vo[1]), "v"(vo[2]), "v"(vo[3]), "v"(vo[4]), "s"(gbase), "s"(lds_dst)
      : "memory");
}
#define SB0() __builtin_amdgcn_sched_barrier(0)
#define XBAR() { SB0(); asm volatile("s_barrier" ::: "memory"); SB0(); }

template <int V> struct IC { static constexpr int value = V; };
template <int I0, int I1, class F>
__device__ __forceinline__ void static_for(F&& f) {
  if constexpr (I0 < I1) { f(IC<I0>{}); static_for<I0 + 1, I1>(f); }
}
template <int J>
__device__ __forceinline__ void dma_piece(const char* gbase  , unsigned vo, unsigned lds_dst  ) {
  if constexpr (J == 0)
    asm volatile("s_mov_b32 m0, %2\n\ts_mov_b32 exec_hi, 0\n\tglobal_load_lds_dwordx4 %0, %1\n\ts_mov_b32 exec_hi, -1"
                 :: "v"(vo), "s"(gbase), "s"(lds_dst) : "memory");
  else
    asm volatile("s_mov_b32 m0, %2\n\tglobal_load_lds_dwordx4 %0, %1 offset:%3"
                 :: "v"(vo), "s"(gbase), "s"(lds_dst), "i"(1024 * J - 512) : "memory");
}
__device__ __forceinline__ f32x4 mma16(u32x4 a, u32x4 b, f32x4 c) {
  return __builtin_amdgcn_mfma_f32_16x16x32_bf16(__builtin_bit_cast(bf16x8, a), __builtin_bit_cast(bf16x8, b), c, 0, 0, 0);
}
template <bool Z>
__device__ __forceinline__ void mma16t(f32x4& c, u32x4 a, u32x4 b) {
  if constexpr (Z) asm volatile("v_mfma_f32_16x16x32_bf16 %0, %1, %2, 0" : "=v"(c) : "v"(a), "v"(b));
  else asm volatile("v_mfma_f32_16x16x32_bf16 %0, %1, %2, %0" : "+v"(c) : "v"(a), "v"(b));
}
#define MFMA_DRAIN() { asm volatile("s_nop 7\n\ts_nop 7" ::: "memory"); SB0(); }
__device__ __forceinline__ float relu(float a) { return __int_as_float(max(__float_as_int(a), 0)); }
__device__ __forceinline__ u32x2 reglu(f32x4 g, f32x4 u) {
  float p[4];
#pragma unroll
  for (int i = 0; i < 4; ++i) asm("v_mul_f32 %0, %1, %2" : "=v"(p[i]) : "v"(relu(g[i])), "v"(u[i]));
  return (u32x2){pk2(p[0], p[1]), pk2(p[2], p[3])};
}

enum { L_NONE = 0, L_X, L_W, L_CC, L_POLL, L_R };
struct K3E {
  int pos, u, k32, tm, off;
  int epi;
  int lk, la, lb;
};
constexpr int K3_N = 342;
struct K3T { K3E e[K3_N]; };
constexpr K3T k3_table() {
  K3T t{};
  int n = 0;
  for (int i = 0; i < 324; ++i) {
    const int u = i / 36, r = i % 36;
    K3E e{};
    e.pos = i; e.u = u; e.epi = -1;
    if (u < 6) {
      e.k32 = 2 * u + r / 18; e.tm = r % 18;
      e.off = (e.tm & 1) * K3_UP + (r / 18) * 9216 + (e.tm >> 1) * 32;
    } else {
      e.k32 = 12 + (r % 12) / 2; e.tm = ((u - 6) * 3 + r / 12) * 2 + (r & 1);
      e.off = (r & 1) * K3_UP + ((r % 12) / 2) * 3072 + (r / 12) * 32;
      if (i > 228 && r % 12 == 2) e.epi = (e.tm >> 1) - 1;
    }
    if (u == 0) {
      if (r < 5) { e.lk = L_W; e.la = 1; e.lb = r; }
      else if (r == 5) e.lk = L_CC;
      else if (r >= 8 && r <= 20 && r % 3 == 2) { e.lk = L_W; e.la = 2; e.lb = (r - 8) / 3; }
      else if (r >= 23 && r <= 32 && r % 3 == 2) { e.lk = L_X; e.la = 8 + (r - 23) / 3; }
    } else if (u == 5) {
      if (r < 4) { e.lk = L_X; e.la = 32 + r; }
      else if (r >= 6 && r <= 18 && r % 3 == 0) { e.lk = L_W; e.la = 7; e.lb = (r - 6) / 3; }
      else if (r == 21) e.lk = L_POLL;
    } else if (u <= 6) {
      if (r <= 12 && r % 3 == 0) { e.lk = L_W; e.la = u + 2; e.lb = r / 3; }
      else if (u < 4 && r >= 15 && r <= 24 && r % 3 == 0) { e.lk = L_X; e.la = 8 + 4 * u + (r - 15) / 3; }
      else if (u == 4 && r >= 15 && r <= 29 && (r & 1)) { e.lk = L_X; e.la = 24 + (r - 15) / 2; }
    } else if (r <= 16 && r % 4 == 0) { e.lk = L_R; e.la = u - 7; e.lb = r / 4; }
    t.e[n++] = e;
    if (u >= 6 && r % 12 == 11) {
      K3E c{};
      c.pos = -1; c.epi = -1;
      c.tm = e.tm - 1; c.off = c.tm * 256; t.e[n++] = c;
      c.tm = e.tm; c.off = c.tm * 256; t.e[n++] = c;
    }
  }
  return t;
}
constexpr K3T K3TAB = k3_table();
constexpr int k3_younger(int J, int lk, int la, int lb) {
  int n = 0;
  for (int j = 0; j < J; ++j) {
    const K3E& e = K3TAB.e[j];
    if (e.epi >= 0) ++n;
    if (e.lk) n += e.lk == L_POLL ? 2 : 1;
    if (e.lk == lk && e.la == la && e.lb == lb) n = 0;
  }
  return n;
}
template <int N> __device__ __forceinline__ void wait_vm() { asm volatile("s_waitcnt vmcnt(%0)" :: "n"(N) : "memory"); }

__device__ __forceinline__ void k3_gemm1(const u16* __restrict__ Wg, const u16* __restrict__ Wu, char* __restrict__ ws, char* sm) {
  const int tid = threadIdx.x, lane = tid & 63;
  const int wave = __builtin_amdgcn_readfirstlane(tid >> 6);
  const int bid = blockIdx.x;
  const int xcd = bid & 7, idx = bid >> 3;
  const int m256 = (xcd >> 2) * 4 + (idx >> 3), sl = (xcd & 3) * 8 + (idx & 7);
  const int mt = m256 * 8 + wave;
  const unsigned voff = lane * 16;
  const unsigned lds0 = (unsigned)(uintptr_t)(LDSP char*)sm;
  const char* wA = (const char*)((wave & 4) ? Wu : Wg) + (size_t)(wave & 3) * 16 * 9216 + sl * 288;
  const char* wB = (const char*)((wave & 4) ? Wu : Wg) + (size_t)(384 + (wave & 3) * 48) * 9216 + sl * 288;
  const unsigned wdst = lds0 + wave * 4608;
  unsigned vo[5];
#pragma unroll
  for (int j = 0; j < 5; ++j) {
    const unsigned off = j ? 1024 * j - 512 : 0, c = off / 16 + lane;
    vo[j] = (c / 18) * 9216 + (c % 18) * 16 - off;
  }
  const char* xsrc = ws + OFF_XS + (size_t)mt * 36864;
  u32x4 X[36];
#define LDX(i) ld_frag<((i) & 3) * 1024>(X[i], xsrc + ((i) >> 2) * 4096, voff)
#define SWP(p, i, q, j) { unsigned a_ = p[i], c_ = q[j]; asm volatile("v_swap_b32 %0, %1" : "+v"(a_), "+v"(c_)); p[i] = a_; q[j] = c_; \
  asm volatile("" : "+v"(p), "+v"(q)); }
#define FIX2(i) { SWP(X[i], 2, X[(i) + 1], 0) SWP(X[i], 3, X[(i) + 1], 1) }
#define FIX4(i) { PIN4(X[i], X[(i) + 1], X[(i) + 2], X[(i) + 3]); FIX2(i) FIX2((i) + 2) \
  asm volatile("s_nop 1" : "+v"(X[i]), "+v"(X[(i) + 1]), "+v"(X[(i) + 2]), "+v"(X[(i) + 3])); }
#define LDX4(i) { LDX(i); LDX((i) + 1); LDX((i) + 2); LDX((i) + 3); }
  dma_rows(wA, vo, wdst + 3 * UNIT);
  LDX4(0)
  XBAR();
  LDX4(4)
  u32x4 sx[9];
  if (wave == 7) {
    const char* sb = ws + OFF_XF + (size_t)(m256 * 8 + (sl >> 2)) * 36864 + (sl & 3) * 64;
    const unsigned so = (lane >> 2) * 256 + (lane & 3) * 16;
#pragma unroll
    for (int i = 0; i < 9; ++i) ld_frag<0>(sx[i], sb + i * 4096, so);
    WAIT_VM(13);
  } else {
    WAIT_VM(4);
  }
  const char* csrc = ws + OFF_CC + (size_t)sl * 6912 + wave * 864;
  const char* w2src = ws + OFF_W2 + (size_t)((xcd & 3) * 4 + (idx & 3)) * 324 * 1024 + wave * 4608;
  const unsigned cdst = lds0 + K3_CCRAW + wave * 864;
  f32x4 acc[36];
  __amdgpu_buffer_rsrc_t hrs = __builtin_amdgcn_make_buffer_rsrc((void*)(ws + OFF_H2 + (size_t)mt * 294912), 0, 294912, 0x00020000);
  const int rw = (lane >> 4) * 4 + ((lane & 15) >> 2);
  lptr bu = (lptr)sm;
  lptr cp = (lptr)sm + K3_CC + (lane & 15) * 16;
  u32x4 A[K3_D];
  unsigned stw[2];
  u32x4 bc[2];
  const char* stp = ws + OFF_ST2 + (size_t)mt * 128;
  auto rd = [&](auto jc, auto hc) -> u32x2 {
    constexpr int H = decltype(hc)::value;
    constexpr K3E E = K3TAB.e[decltype(jc)::value];
    if constexpr (E.pos >= 0) {
      constexpr int RS = E.u < 6 ? 288 : 96;
      if constexpr (E.pos % 36 == 0 && H == 0) { bu = (lptr)sm + ((E.u + 3) & 3) * UNIT + rw * RS + (lane & 3) * 8; asm volatile("" : "+v"(bu)); }
      return __builtin_bit_cast(u32x2, __builtin_amdgcn_ds_read_tr16_b64_v4i16((LDSP s16x4*)(bu + E.off + H * 16 * RS)));
    } else {
      return *(LDSP const u32x2*)(cp + E.off + H * 8);
    }
  };
  auto epi = [&](auto tc) {
    constexpr int T = decltype(tc)::value, c = T * 4;
    const u32x2 h0 = reglu(acc[c], acc[c + 2]), h1 = reglu(acc[c + 1], acc[c + 3]);
    __builtin_amdgcn_raw_buffer_store_b128((u32x4){h0[0], h0[1], h1[0], h1[1]}, hrs, voff, (sl * 9 + T) * 1024, H2_AUX);
    SB0();
  };
#define POLL() asm volatile("global_load_dword %0, %2, %3 sc1\n\tglobal_load_dword %1, %2, %3 offset:64 sc1" \
                            : "=&v"(stw[0]), "=&v"(stw[1]) : "v"((lane & 15) * 4), "s"(stp) : "memory")
  FIX4(0)
  XBAR();
  static_for<0, K3_D>([&](auto jc) {
    const u32x2 n0 = rd(jc, IC<0>{}), n1 = rd(jc, IC<1>{});
    A[decltype(jc)::value] = (u32x4){n0[0], n0[1], n1[0], n1[1]};
  });
  if (wave == 7) {
    WAIT_VM(0);
    PIN4(sx[0], sx[1], sx[2], sx[3]); PIN4(sx[4], sx[5], sx[6], sx[7]); asm volatile("" : "+v"(sx[8]));
    const bf16x2 ones = __builtin_bit_cast(bf16x2, 0x3f803f80u);
    float s1[2] = {0.f, 0.f}, s2[2] = {0.f, 0.f};
#pragma unroll
    for (int i = 0; i < 9; ++i)
#pragma unroll
      for (int e = 0; e < 4; ++e) {
        const unsigned xu = sx[i][e];
        const bf16x2 xx = __builtin_bit_cast(bf16x2, xu);
        s1[e >> 1] = __builtin_amdgcn_fdot2_f32_bf16(xx, ones, s1[e >> 1], false);
        s2[e >> 1] = __builtin_amdgcn_fdot2_f32_bf16(xx, xx, s2[e >> 1], false);
      }
    const bool up = lane & 4;
    float a = (up ? s1[1] : s1[0]) + __shfl_xor(up ? s1[0] : s1[1], 4), b = (up ? s2[1] : s2[0]) + __shfl_xor(up ? s2[0] : s2[1], 4);
#pragma unroll
    for (int o = 8; o < 64; o <<= 1) { a += __shfl_xor(a, o); b += __shfl_xor(b, o); }
    const float mean = a * (1.f / 576.f);
    const float var = fmaxf(b * (1.f / 576.f) - mean * mean, 0.f);
    const unsigned w = pk2(mean, sqrtf(bfr(bfr(var) + 1e-5f)));
    if (lane < 8)
      __hip_atomic_store((GLBP unsigned*)(ws + OFF_ST2) + m256 * 256 + (sl >> 2) * 32 + (sl & 3) * 4 + (lane & 3) + (lane & 4) * 4, w, __ATOMIC_RELAXED, __HIP_MEMORY_SCOPE_AGENT);
  }
  SB0();
  static_for<0, K3_N>([&](auto jc) {
    constexpr int J = decltype(jc)::value;
    constexpr K3E E = K3TAB.e[J];
    constexpr int I = E.pos;
    if constexpr (I >= 0 && I % 36 == 30 && I < 300) {
      wait_vm<k3_younger(J, I == 30 ? L_CC : L_W, I == 30 ? 0 : E.u + 1, I == 30 ? 0 : 4)>();
      XBAR();
      TS_STEP(E.u + 2)
    }
    if constexpr (I == 32) {
      if (tid < 288) {
        const float* rp = (const float*)(sm + K3_CCRAW) + tid;
        const float c1 = rp[0] + rp[576] + rp[1152], c2 = rp[288] + rp[864] + rp[1440];
        const float h1 = bfr(c1), r1 = c1 - h1, m1 = bfr(r1), h2 = bfr(c2), r2 = c2 - h2, m2 = bfr(r2);
        *(u32x4*)(sm + K3_CC + tid * 16) = (u32x4){pk2(h1, m1), pk2(r1 - m1, h2), pk2(m2, r2 - m2), 0u};
      }
      SB0();
    }
    if constexpr (I > 0 && (I <= 216 ? I % 36 == 0 : (I == 220 || I == 224))) {
      constexpr int x0 = E.k32 * 2;
      wait_vm<k3_younger(J, L_X, x0 + 3, 0)>();
      FIX4(x0)
      SB0();
    }
    if constexpr (I == 130) {
#pragma unroll
      for (int j = 0; j < 5; ++j) {
        const unsigned off = j ? 1024 * j - 512 : 0, c = off / 16 + lane;
        vo[j] = (c / 6) * 9216 + (c % 6) * 16 - off;
      }
      SB0();
    }
    if constexpr (I == 226) {
      wait_vm<k3_younger(J, L_POLL, 0, 0)>();
      asm volatile("" : "+v"(stw[0]), "+v"(stw[1]));
      for (unsigned spins = 0; __builtin_amdgcn_ballot_w64(stw[0] == 0u || stw[1] == 0u) != 0ull && spins < (1u << 20); ++spins) {
        __builtin_amdgcn_s_sleep(2);
        POLL(); WAIT_VM(0); asm volatile("" : "+v"(stw[0]), "+v"(stw[1]));
      }
#pragma unroll
      for (int m = 0; m < 2; ++m) {
        const unsigned w = lane < 16 ? (stw[m] ^ 0x8000u) : 0u;
        const unsigned nm = w & 0xffffu, sd = w >> 16;
        bc[m] = (u32x4){nm | (nm << 16), w, sd | (sd << 16), 0u};
      }
      asm volatile("" : "+v"(bc[0]), "+v"(bc[1]));
      SB0();
    }
    if constexpr (E.epi >= 0) epi(IC<E.epi>{});
    const u32x4 a = A[J % K3_D];
    constexpr bool Z = I >= 0 && I < 18;
    u32x2 n0, n1;
    if constexpr (I >= 0) mma16t<Z>(acc[2 * E.tm], a, X[E.k32 * 2]); else mma16t<false>(acc[2 * E.tm], a, bc[0]);
    if constexpr (J + K3_D < K3_N) n0 = rd(IC<J + K3_D>{}, IC<0>{});
    SB0();
    if constexpr (I >= 0) mma16t<Z>(acc[2 * E.tm + 1], a, X[E.k32 * 2 + 1]); else mma16t<false>(acc[2 * E.tm + 1], a, bc[1]);
    if constexpr (J + K3_D < K3_N) { n1 = rd(IC<J + K3_D>{}, IC<1>{}); A[J % K3_D] = (u32x4){n0[0], n0[1], n1[0], n1[1]}; }
    if constexpr (E.lk == L_X) LDX(E.la);
    if constexpr (E.lk == L_W)
      dma_piece<E.lb>(E.la < 6 ? wA + (size_t)E.la * 64 * 9216 : wB + (E.la - 6) * 96, vo[E.lb], wdst + ((E.la + 3) & 3) * UNIT);
    if constexpr (E.lk == L_R) dma_piece<E.lb>(w2src + E.la * UNIT, voff, wdst + E.la * UNIT);
    if constexpr (E.lk == L_CC)
      asm volatile("s_mov_b32 m0, %2\n\ts_mov_b32 exec_hi, 0x3fffff\n\tglobal_load_lds_dwordx4 %0, %1\n\ts_mov_b32 exec_hi, -1"
                   :: "v"(voff), "s"(csrc), "s"(cdst) : "memory");
    if constexpr (E.lk == L_POLL) POLL();
    SB0();
  });
  MFMA_DRAIN();
  epi(IC<8>{});
}

constexpr int K4_LDS = 4 * UNIT + 16384;
constexpr int K4_N = 162, K4_D = 6;
#define K4_PRE3 1
struct K4E { int u, q, hb, b, off, lk, la, lb; };
struct K4T { K4E e[K4_N]; };
constexpr K4T k4_table() {
  K4T t{};
  for (int i = 0; i < K4_N; ++i) {
    const int u = i / 18, r = i % 18;
    K4E e{};
    e.u = u;
    e.q = u < 6 ? r % 9 : (u - 6) * 3 + r / 6;
    e.hb = u < 6 ? 2 * u + r / 9 : 12 + r % 6;
    e.b = e.q == 6 ? 2 : (e.q < 6 ? e.q & 1 : e.q - 7);
    const int pb = e.q == 6 ? 6 : e.q - e.b;
    e.off = u < 6 ? ((r / 9) * 18 + pb) * 1024 : ((u == 8 ? pb - 6 : pb) * 12 + 2 * (r % 6)) * 1024;
    if (u == 0) {
      if (r % 3 != 2) { e.lk = L_X; e.la = 8 + r - r / 3; }
      else if (!K4_PRE3 && r < 15) { e.lk = L_W; e.la = 3; e.lb = r / 3; }
    } else if (u <= 5) {
      if (r <= 8 && !(r & 1)) { e.lk = L_W; e.la = u + 3; e.lb = r / 2; }
      else if (u <= 4 && r >= 10 && !(r & 1)) { e.lk = L_X; e.la = 16 + 4 * u + (r - 10) / 2; }
      else if (u == 5 && r >= 10 && r < 17) { e.lk = L_R; e.la = r - 10; }
    }
    t.e[i] = e;
  }
  return t;
}
constexpr K4T K4TAB = k4_table();
constexpr int k4_send(int i) { return i == 122 ? 0 : i == 134 ? 1 : i == 146 ? 2 : -1; }
constexpr int k4_younger(int J, int lk, int la, int lb, int pre) {
  int n = pre;
  for (int j = 0; j < J; ++j) {
    const K4E& e = K4TAB.e[j];
    if (k4_send(j) >= 0) ++n;
    if (e.lk) ++n;
    if (e.lk == lk && e.la == la && e.lb == lb) n = 0;
  }
  return n;
}
constexpr int k4_xoff(int j) { return j < 2 ? UNIT + j * 16384 : j == 2 ? 4 * UNIT : 2 * UNIT + (j - 3) * 16384; }
__device__ __forceinline__ f32x4 add4(f32x4 a, f32x4 b) {
  f32x4 r;
#pragma unroll
  for (int i = 0; i < 4; ++i) asm("v_add_f32 %0, %1, %2" : "=v"(r[i]) : "v"(a[i]), "v"(b[i]));
  return r;
}

__global__ __launch_bounds__(512, 2) void k34_mlp(const u16* __restrict__ Wg, const u16* __restrict__ Wu, char* __restrict__ ws,
                                                  u16* __restrict__ out) {
  extern __shared__ __attribute__((aligned(16))) char sm[];
  int wave = __builtin_amdgcn_readfirstlane(threadIdx.x >> 6), bid = blockIdx.x;
  k3_gemm1(Wg, Wu, ws, sm);
  int lane;
  asm volatile("v_mbcnt_lo_u32_b32 %0, -1, 0\n\tv_mbcnt_hi_u32_b32 %0, -1, %0\n\ts_waitcnt vmcnt(0)" : "=v"(lane), "+s"(wave), "+s"(bid), "+s"(ws) :: "memory");
  const int tid = wave * 64 + lane;
  GLBP unsigned* grp = (GLBP unsigned*)(ws + OFF_FLG + ((bid >> 6) * 8 + (bid & 7)) * 512);
  XBAR();
  const int wm = wave & 3, wk = wave >> 2;
  const int xcd = bid & 7, idx = bid >> 3;
  const int kb = xcd & 3;
  const int m128 = (xcd >> 2) * 8 + (idx >> 2), nt = idx & 3;
  const unsigned voff = lane * 16;
  const unsigned lds0 = (unsigned)(uintptr_t)(LDSP char*)sm;
  const char* wsrc = ws + OFF_W2 + (size_t)(kb * 4 + nt) * 324 * 1024 + wave * 4608;
  const unsigned wdst = lds0 + wave * 4608;
  const char* h0 = ws + OFF_H2 + ((size_t)(m128 * 4 + wm) * 288 + (kb * 36 + wk) * 2) * 1024;
  u32x4 H[36];
#define LDH(i) ld_frag<((i) & 1) * 1024, H_SC1>(H[i], h0 + ((i) >> 1) * 4096, voff)
  if (tid == 0) __hip_atomic_fetch_add(grp, 1u, __ATOMIC_RELAXED, __HIP_MEMORY_SCOPE_AGENT);
  dma_unit(wsrc + 2 * UNIT, voff, wdst + 2 * UNIT);
  dma_unit(wsrc + 3 * UNIT, voff, wdst + 3 * UNIT);
  if (wave == 0) {
    for (unsigned spins = 0; spins < (1u << 22); ++spins) {
      if ((unsigned)__builtin_amdgcn_readfirstlane(__hip_atomic_load(grp, __ATOMIC_RELAXED, __HIP_MEMORY_SCOPE_AGENT)) >= 8u) break;
      __builtin_amdgcn_s_sleep(1);
    }
  }
  XBAR();
  LDH(0); LDH(1); LDH(2); LDH(3);
  LDH(4); LDH(5); LDH(6); LDH(7);
  f32x4 acc[18];
  u32x4 A[K4_D];
  lptr bu[3];
  LDSP char* xw = (LDSP char*)sm + wave * 2048 + lane * 16;
  lptr xr_ = (lptr)sm + (wave ^ 4) * 2048 + lane * 16;
  const int tile = m128 * 4 + nt;
  __amdgpu_buffer_rsrc_t prs = __builtin_amdgcn_make_buffer_rsrc((void*)(ws + OFF_PART + (size_t)tile * PART_TILE), 0, PART_TILE, 0x00020000);
  f32x4 oth[2], f8;
  const int r0 = m128 * 128 + wm * 32, dA = nt * 9 + 2 * kb + wk, d8 = nt * 9 + 8;
  const char* xfA = ws + OFF_XF + (size_t)(r0 >> 5) * 36864 + dA * 1024;
  const char* xf8 = ws + OFF_XF + (size_t)(r0 >> 5) * 36864 + d8 * 1024 + wk * 8;
  const char* gbp = ws + OFF_MOD + 5 * 576 * 4;
  const char* stp = ws + OFF_ST2 + r0 * 4;
  u32x2 xres[3];
  u32x4 gbv[2];
  unsigned stw[2];
  auto rd = [&](auto jc) {
    constexpr int J = decltype(jc)::value;
    constexpr K4E E = K4TAB.e[J];
    if constexpr (J % 18 == 0 && E.u != 7) {
      constexpr int ST = E.u < 6 ? 1024 : 12288;
      bu[2] = (lptr)sm + (E.u & 3) * UNIT + wk * (E.u < 6 ? 9216 : 1024) + lane * 16;
      bu[1] = bu[2] + wk * ST; bu[0] = bu[2] + (1 - wk) * ST;
      asm volatile("" : "+v"(bu[0]), "+v"(bu[1]), "+v"(bu[2]));
    }
    return *(LDSP const u32x4*)(bu[E.b] + E.off);
  };
  WAIT_VM(4);
  XBAR();
  static_for<0, K4_D>([&](auto jc) { A[decltype(jc)::value] = rd(jc); });
  SB0();
  static_for<0, K4_N>([&](auto jc) {
    constexpr int J = decltype(jc)::value;
    constexpr K4E E = K4TAB.e[J];
    constexpr int R = J - 108;
    if constexpr (J % 18 == 12 && J < 108) {
      wait_vm<k4_younger(J, L_W, E.u + 1, 4, 8)>();
      asm volatile("s_waitcnt lgkmcnt(0)" ::: "memory");
      XBAR();
      TS_STEP(E.u + 2)
    }
    if constexpr (R == 3) { wait_vm<k4_younger(J, L_W, 7, 4, 0)>(); XBAR(); }
    if constexpr (R == 7 || R == 19 || R == 31 || R == 49) {
      constexpr int j = R == 49 ? 4 : R / 12, q = E.q - 1;
      *(LDSP f32x4*)(xw + k4_xoff(j)) = acc[2 * q];
      *(LDSP f32x4*)(xw + k4_xoff(j) + 1024) = acc[2 * q + 1];
      SB0();
    }
    if constexpr (R == 43) {
      if (wk) { *(LDSP f32x4*)(xw + k4_xoff(3)) = acc[12]; f8 = acc[13]; } else { *(LDSP f32x4*)(xw + k4_xoff(3)) = acc[13]; f8 = acc[12]; }
      SB0();
    }
    if constexpr (R == 9 || R == 21 || R == 33 || R == 45 || R == 51) {
      if constexpr (R == 21) wait_vm<k4_younger(J, L_W, 8, 4, 0)>();
      asm volatile("s_waitcnt lgkmcnt(%0)" :: "n"(R < 48 ? 2 : 0) : "memory");
      XBAR();
      if constexpr (R == 9 || R == 33) { TS_STEP(R == 9 ? 8 : 9) }
      constexpr int j = R == 51 ? 4 : R / 12;
      oth[0] = *(LDSP const f32x4*)(xr_ + k4_xoff(j));
      if constexpr (j != 3) oth[1] = *(LDSP const f32x4*)(xr_ + k4_xoff(j) + 1024);
      SB0();
    }
    if constexpr (k4_send(J) >= 0) {
      constexpr int j = k4_send(J);
      const f32x4 s0 = add4(acc[4 * j + 2], oth[0]), s1 = add4(acc[4 * j + 3], oth[1]);
      __builtin_amdgcn_raw_buffer_store_b128((u32x4){pk2(s0[0], s0[1]), pk2(s0[2], s0[3]), pk2(s1[0], s1[1]), pk2(s1[2], s1[3])}, prs, voff,
                                             ((((kb + j + 1) & 3) * 4 + kb) * 8 + wave) * 1024, 16);
      SB0();
    }
    if constexpr (R == 47) {
      f8 = add4(f8, oth[0]);
      if (wm != kb)
        __builtin_amdgcn_raw_buffer_store_b64((u32x2){pk2(f8[0], f8[1]), pk2(f8[2], f8[3])}, prs, lane * 8, 131072 + ((wm * 4 + kb) * 2 + wk) * 512, 16);
      SB0();
    }
    constexpr bool first = E.u < 6 ? E.q == 0 : J < 114;
    if constexpr (first) {
      constexpr int hb = E.hb;
      if constexpr (hb >= 2) wait_vm<k4_younger(J, L_X, 2 * hb + 1, 0, hb == 2 ? 2 : 0)>();
      asm volatile("" : "+v"(H[2 * hb]), "+v"(H[2 * hb + 1]));
      const u32x4 l0 = H[2 * hb], l1 = H[2 * hb + 1];
      H[2 * hb] = (u32x4){l0[0], l0[1], l1[0], l1[1]};
      H[2 * hb + 1] = (u32x4){l0[2], l0[3], l1[2], l1[3]};
      asm volatile("s_nop 1" : "+v"(H[2 * hb]), "+v"(H[2 * hb + 1]));
      SB0();
    }
    const u32x4 a = A[J % K4_D];
    mma16t<(J < 9)>(acc[2 * E.q], a, H[2 * E.hb]);
    if constexpr (J + K4_D < K4_N) A[J % K4_D] = rd(IC<J + K4_D>{});
    SB0();
    mma16t<(J < 9)>(acc[2 * E.q + 1], a, H[2 * E.hb + 1]);
    if constexpr (E.lk == L_X) LDH(E.la);
    if constexpr (E.lk == L_W) dma_piece<E.lb>(wsrc + E.la * UNIT, voff, wdst + (E.la & 3) * UNIT);
    if constexpr (E.lk == L_R) {
      if constexpr (E.la < 3)
        asm volatile("global_load_dwordx2 %0, %1, %2" : "=v"(xres[E.la]) : "v"(voff), "s"(E.la == 2 ? xf8 : xfA + E.la * 8) : "memory");
      else if constexpr (E.la < 5)
        asm volatile("global_load_dwordx4 %0, %1, %2" : "=v"(gbv[E.la - 3]) : "v"(lane & 48), "s"(gbp + (E.la == 3 ? dA : d8) * 64) : "memory");
      else
        asm volatile("global_load_dword %0, %1, %2 sc1" : "=v"(stw[E.la - 5]) : "v"((lane & 15) * 4), "s"(stp + (E.la - 5) * 64) : "memory");
    }
    SB0();
  });
  WAIT_VM(0);
  asm volatile("" : "+v"(xres[0]), "+v"(xres[1]), "+v"(xres[2]), "+v"(gbv[0]), "+v"(gbv[1]), "+v"(stw[0]), "+v"(stw[1]));
  XBAR();
  GLBP unsigned* cnt = (GLBP unsigned*)(ws + OFF_CNT + tile * 128);
  if (tid == 0) __hip_atomic_fetch_add(cnt, 1u, __ATOMIC_RELAXED, __HIP_MEMORY_SCOPE_AGENT);
  MFMA_DRAIN();
  f32x4 fin[3] = {add4(acc[16], oth[0]), add4(acc[17], oth[1]), f8};
  float rs2[3];
#pragma unroll
  for (int i = 0; i < 2; ++i) { const float r = 1.f / hi16(stw[i]); rs2[i] = r * r; }
  rs2[2] = wk ? rs2[1] : rs2[0];
  if (wave == 0) {
    for (unsigned spins = 0; spins < (1u << 22); ++spins) {
      if ((unsigned)__builtin_amdgcn_readfirstlane(__hip_atomic_load(cnt, __ATOMIC_RELAXED, __HIP_MEMORY_SCOPE_AGENT)) >= 4u) break;
      __builtin_amdgcn_s_sleep(1);
    }
  }
  BARRIER();
  asm volatile(".Lk34_tail:");
  __amdgpu_buffer_rsrc_t ors = __builtin_amdgcn_make_buffer_rsrc((void*)out, 0, S_ * D_ * 2, 0x00020000);
  u32x4 pp[3];
  u32x2 p8[3];
#pragma unroll
  for (int j = 0; j < 3; ++j) {
    const int src = (kb + 1 + j) & 3;
    pp[j] = __builtin_amdgcn_raw_buffer_load_b128(prs, voff, ((kb * 4 + src) * 8 + wave) * 1024, 16);
    p8[j] = __builtin_amdgcn_raw_buffer_load_b64(prs, lane * 8, 131072 + ((kb * 4 + src) * 2 + wk) * 512, 16);
  }
#pragma unroll
  for (int i = 0; i < 3; ++i) {
    if (i == 2 && wm != kb) break;
    u32x2 o;
#pragma unroll
    for (int e = 0; e < 2; ++e) {
      const unsigned q0 = i < 2 ? pp[0][2 * i + e] : p8[0][e], q1 = i < 2 ? pp[1][2 * i + e] : p8[1][e], q2 = i < 2 ? pp[2][2 * i + e] : p8[2][e];
      float a = ((fin[i][2 * e] + lo16(q0)) + (lo16(q1) + lo16(q2))) * rs2[i];
      float b = ((fin[i][2 * e + 1] + hi16(q0)) + (hi16(q1) + hi16(q2))) * rs2[i];
      bfr2(a, b);
      const unsigned g0 = gbv[i >> 1][2 * e], g1 = gbv[i >> 1][2 * e + 1];
      a *= __uint_as_float(g0); b *= __uint_as_float(g1); bfr2(a, b);
      o[e] = pk2(lo16(xres[i][e]) + a, hi16(xres[i][e]) + b);
    }
    const int m = r0 + (i < 2 ? i : wk) * 16 + (lane & 15);
    __builtin_amdgcn_raw_buffer_store_b64(o, ors, (m * D_ + (i < 2 ? dA : d8) * 16 + (lane >> 4) * 4) * 2, 0, 2);
  }
}

extern "C" void kernel_launch(void* const* d_in, const int* in_sizes, int n_in, void* d_out, int out_size,
                              void* d_ws, size_t ws_size, hipStream_t stream) {
  const u16* A_q = (const u16*)d_in[0];
  const u16* A_k = (const u16*)d_in[1];
  const u16* A_v = (const u16*)d_in[2];
  const u16* A_sh = (const u16*)d_in[3];
  const u16* A_sk = (const u16*)d_in[4];
  const u16* A_D = (const u16*)d_in[5];
  const u16* Wg = (const u16*)d_in[7];
  const u16* Wu = (const u16*)d_in[8];
  const u16* Wd = (const u16*)d_in[9];
  const u16* cv = (const u16*)d_in[10];
  const u16* wm = (const u16*)d_in[11];
  char* ws = (char*)d_ws;
  (void)hipFuncSetAttribute((const void*)k1_conv, hipFuncAttributeMaxDynamicSharedMemorySize, K1_LDS);
  (void)hipFuncSetAttribute((const void*)k34_mlp, hipFuncAttributeMaxDynamicSharedMemorySize, K4_LDS);
  k0_prep<<<K0_GRID, 256, 0, stream>>>(A_q, A_k, A_v, A_sh, cv, wm, A_sk, ws);
  k1_conv<<<K1_CONV + K1_W, 512, K1_LDS, stream>>>(A_q, A_sk, A_D, Wg, Wu, Wd, ws);
  k34_mlp<<<256, 512, K4_LDS, stream>>>(Wg, Wu, ws, (u16*)d_out);
}
```

```cpp
#include <hip/hip_runtime.h>
#include <hip/hip_bf16.h>
#include <stdint.h>

typedef unsigned short u16;
typedef __attribute__((ext_vector_type(2))) __bf16 bf16x2;
typedef __attribute__((ext_vector_type(8))) __bf16 bf16x8;
typedef __attribute__((ext_vector_type(2))) float f32x2;
typedef __attribute__((ext_vector_type(4))) float f32x4;
typedef __attribute__((ext_vector_type(16))) float f32x16;
typedef __attribute__((ext_vector_type(4))) unsigned u32x4;
typedef __attribute__((ext_vector_type(2))) unsigned u32x2;
typedef __attribute__((ext_vector_type(4))) short s16x4;
#define LDSP __attribute__((address_space(3)))
#define GLBP __attribute__((address_space(1)))

constexpr int S_ = 2048, D_ = 576, F_ = 4608;

constexpr size_t OFF_CNT = 0;
constexpr size_t OFF_MOD = 8192;
constexpr size_t OFF_ST1 = OFF_MOD + 16384;
constexpr size_t OFF_ST2 = OFF_ST1 + 8192;
constexpr size_t OFF_CC  = OFF_ST2 + 8192;
constexpr size_t OFF_UT  = OFF_CC + 32 * 6912;
constexpr size_t OFF_FLG = OFF_UT + (size_t)D_ * S_ * 2;
constexpr size_t OFF_XF  = OFF_FLG + 16384;
constexpr size_t OFF_XS  = OFF_XF + (size_t)D_ * S_ * 2;
constexpr size_t OFF_W2  = OFF_XS + (size_t)D_ * S_ * 2;
constexpr size_t OFF_H2  = OFF_W2 + (size_t)D_ * F_ * 2;
constexpr size_t OFF_PART = OFF_H2 + (size_t)S_ * F_ * 2;
constexpr int PART_TILE = 131072 + 16384;

__device__ __forceinline__ unsigned pk2(float a, float b) {
  f32x2 v = {a, b};
  return __builtin_bit_cast(unsigned, __builtin_convertvector(v, bf16x2));
}
__device__ __forceinline__ float lo16(unsigned u) { return __uint_as_float(u << 16); }
__device__ __forceinline__ float hi16(unsigned u) { return __uint_as_float(u & 0xffff0000u); }
__device__ __forceinline__ float bfr(float a) { return lo16(pk2(a, a)); }
__device__ __forceinline__ void bfr2(float& a, float& b) { unsigned u = pk2(a, b); a = lo16(u); b = hi16(u); }
__device__ __forceinline__ float bf2f(u16 x) { return __uint_as_float(((unsigned)x) << 16); }
__device__ __forceinline__ s16x4 tr_read(const char* p) {
  return __builtin_amdgcn_ds_read_tr16_b64_v4i16((LDSP s16x4*)p);
}
__device__ __forceinline__ u32x4 tr_read2(const char* p0, const char* p1) {
  s16x4 a = tr_read(p0), b = tr_read(p1);
  u32x2 x = __builtin_bit_cast(u32x2, a), y = __builtin_bit_cast(u32x2, b);
  u32x4 r = {x[0], x[1], y[0], y[1]};
  return r;
}

#define BARRIER() { asm volatile("s_waitcnt lgkmcnt(0)" ::: "memory"); __builtin_amdgcn_s_barrier(); asm volatile("" ::: "memory"); }
#define PIN4(a, b, c, d) asm volatile("" : "+v"(a), "+v"(b), "+v"(c), "+v"(d))
typedef LDSP const char* lptr;

#define CODE_TAIL 1900
#define NT 0
template <int B> __device__ __forceinline__ u32x4 ldv(const void* p) {
  if constexpr ((NT >> B) & 1) return __builtin_nontemporal_load((const u32x4*)p); else return *(const u32x4*)p;
}
#define W2_AUX 16
__device__ __forceinline__ void wd_load(u32x4 (&wd)[9], const u16* Wd, int pc, int lane) {
#pragma unroll
  for (int i = 0; i < 9; ++i) {
    const int c = i * 64 + lane;
    wd[i] = ldv<3>((const char*)Wd + (size_t)(pc >> 2) * 36864 + (pc & 3) * 288 + (c / 18) * 1152 + (c % 18) * 16);
  }
}
__device__ __forceinline__ void wd_store(const u32x4 (&wd)[9], char* im, char* ws, int pc, int lane) {
#pragma unroll
  for (int i = 0; i < 9; ++i) *(u32x4*)(im + i * 1024 + lane * 16) = wd[i];
  const char* base = im + ((lane >> 4) * 4 + ((lane & 15) >> 2)) * 288 + (lane & 3) * 8;
  const int kb = (pc >> 2) / 36, Tl = (pc >> 2) % 36;
  __amdgpu_buffer_rsrc_t wrs = __builtin_amdgcn_make_buffer_rsrc((void*)(ws + OFF_W2 + (size_t)(kb * 4 + (pc & 3)) * 331776), 0, 331776, 0x00020000);
  asm volatile("s_waitcnt lgkmcnt(0)" ::: "memory");
#pragma unroll
  for (int i = 0; i < 9; ++i) {
    const u32x4 r = tr_read2(base + i * 32, base + i * 32 + 16 * 288);
    const int o = ((i >> 1) - kb) & 3;
    const int pos = i == 8 ? 6 : (o ? 2 * (o - 1) + (i & 1) : 7 + (i & 1));
    __builtin_amdgcn_raw_buffer_store_b128(r, wrs, lane * 16, (Tl < 24 ? Tl * 9 + pos : 216 + pos * 12 + Tl - 24) * 1024, W2_AUX);
  }
  __builtin_amdgcn_sched_barrier(0);
  asm volatile("s_nop 1" ::: "memory");
}
#define K0_AUX 16
constexpr int K0_MOD = 108, K0_KV = 288, K0_ST = 64;
constexpr int K0_GRID = K0_MOD + K0_KV + K0_ST;

__global__ __launch_bounds__(256) void k0_prep(const u16* __restrict__ xq, const u16* __restrict__ kk,
                                               const u16* __restrict__ vv, const u16* __restrict__ shift_k,
                                               const u16* __restrict__ cvec, const u16* __restrict__ w_mod,
                                               const u16* __restrict__ ssm_k, char* __restrict__ ws) {
  __shared__ __attribute__((aligned(16))) char sm[8192];
  const int tid = threadIdx.x, lane = tid & 63, wave = tid >> 6;
  int bid = blockIdx.x;
  if (bid < K0_MOD) {
    if (bid == 0 && tid < 64) *(unsigned*)(ws + OFF_CNT + tid * 128) = 0u;
    if (bid == 9) *(unsigned*)(ws + OFF_FLG + tid * 64) = 0u;
    if (bid >= 1 && bid <= 8) ((unsigned*)(ws + OFF_ST2))[(bid - 1) * 256 + tid] = 0u;
    const int c4 = tid & 3, rg = tid >> 2;
    bid = (bid >= 100 && bid < 104) ? bid + 4 : (bid & ~15) + (bid & 7) * 2 + ((bid >> 3) & 1);
    u32x4 w[9];
    float sc[9];
#pragma unroll
    for (int i = 0; i < 9; ++i) {
      const int row = rg + 64 * i;
      w[i] = ldv<0>(w_mod + (size_t)row * 3456 + bid * 32 + c4 * 8);
      sc[i] = bf2f(cvec[row]);
    }
    float acc[8];
#pragma unroll
    for (int e = 0; e < 8; ++e) acc[e] = 0.f;
#pragma unroll
    for (int i = 0; i < 9; ++i) {
      const float s = bfr(sc[i] / (1.f + __expf(-sc[i])));
#pragma unroll
      for (int e = 0; e < 4; ++e) {
        acc[2 * e] += s * lo16(w[i][e]);
        acc[2 * e + 1] += s * hi16(w[i][e]);
      }
    }
#pragma unroll
    for (int e = 0; e < 8; ++e) {
#pragma unroll
      for (int o = 4; o < 64; o <<= 1) acc[e] += __shfl_xor(acc[e], o);
    }
    float* red = (float*)sm;
    if (lane < 4) {
#pragma unroll
      for (int e = 0; e < 8; ++e) red[wave * 32 + lane * 8 + e] = acc[e];
    }
    __syncthreads();
    if (tid < 32) {
      const float r = (red[tid] + red[32 + tid]) + (red[64 + tid] + red[96 + tid]);
      ((float*)(ws + OFF_MOD))[bid * 32 + tid] = bfr(r);
    }
    return;
  }
  bid -= K0_MOD;
  if (bid < K0_KV) {
    const int cb = bid % 9, tb = bid / 9;
    const int c8 = tid & 7, rp = tid >> 3;
    const int r0 = tb * 64 + rp * 2;
    const int ch0 = cb * 64 + c8 * 8;
    u32x4 kr[5], vr[2], sw[4];
    unsigned warm = 0;
    {
      const char* pc;
      unsigned len;
      asm volatile("s_getpc_b64 %0\n.Lk0_here:\n\ts_mov_b32 %1, .Lk34_tail-.Lk0_here" : "=s"(pc), "=s"(len));
      const unsigned off = (bid >> 3) * 8192 + (tid & 127) * 64;
      const char* wp = tid < 128 ? (const char*)ssm_k + (blockIdx.x & 7) * 294912 + off : pc + off;
      if (tid < 128 || off < len + CODE_TAIL) asm volatile("global_load_dword %0, %1, off" : "+v"(warm) : "v"(wp) : "memory");
    }
#pragma unroll
    for (int i = 0; i < 5; ++i) {
      const int r = r0 - 3 + i;
      const int rc = r < 0 ? 0 : r;
      kr[i] = ldv<1>(kk + (size_t)rc * D_ + ch0);
      if (r < 0) kr[i] = (u32x4){0u, 0u, 0u, 0u};
    }
#pragma unroll
    for (int i = 0; i < 2; ++i) vr[i] = ldv<2>(vv + (size_t)(r0 + i) * D_ + ch0);
#pragma unroll
    for (int i = 0; i < 4; ++i) sw[i] = *(const u32x4*)(shift_k + (size_t)ch0 * 4 + i * 8);
    char* tu = sm;
    const int q = rp >> 2, inner = rp & 3;
#pragma unroll
    for (int e = 0; e < 8; ++e) {
      const unsigned wa = sw[e >> 1][(e & 1) * 2], wb = sw[e >> 1][(e & 1) * 2 + 1];
      const float w0 = lo16(wa), w1 = hi16(wa), w2 = lo16(wb), w3 = hi16(wb);
      float kf[5];
#pragma unroll
      for (int i = 0; i < 5; ++i) kf[i] = (e & 1) ? hi16(kr[i][e >> 1]) : lo16(kr[i][e >> 1]);
      const float v0 = (e & 1) ? hi16(vr[0][e >> 1]) : lo16(vr[0][e >> 1]);
      const float v1 = (e & 1) ? hi16(vr[1][e >> 1]) : lo16(vr[1][e >> 1]);
      float a0 = kf[0] * w3, a1 = kf[1] * w3; bfr2(a0, a1);
      float t0 = kf[1] * w2, t1 = kf[2] * w2; bfr2(t0, t1);
      a0 += t0; a1 += t1; bfr2(a0, a1);
      t0 = kf[2] * w1; t1 = kf[3] * w1; bfr2(t0, t1);
      a0 += t0; a1 += t1; bfr2(a0, a1);
      t0 = kf[3] * w0; t1 = kf[4] * w0; bfr2(t0, t1);
      a0 += t0; a1 += t1; bfr2(a0, a1);
      const unsigned kvp = pk2(a0 * v0, a1 * v1);
      const int off = (c8 * 8 + e) * 128 + ((q ^ c8) * 16) + inner * 4;
      *(unsigned*)(tu + off) = kvp;
    }
    __syncthreads();
    asm volatile("s_waitcnt vmcnt(0)" : "+v"(warm));
    __amdgpu_buffer_rsrc_t urs = __builtin_amdgcn_make_buffer_rsrc((void*)(ws + OFF_UT), 0, D_ * S_ * 2, 0x00020000);
#pragma unroll
    for (int i = 0; i < 2; ++i) {
      const int p = tid + 256 * i, ch = p >> 3, qq = p & 7;
      const int off = ch * 128 + ((qq ^ (ch >> 3)) * 16);
      __builtin_amdgcn_raw_buffer_store_b128(*(const u32x4*)(tu + off), urs, ((cb * 64 + ch) * S_ + tb * 64 + qq * 8) * 2, 0, K0_AUX);
    }
    return;
  }
  bid -= K0_KV;
  {
    const int row = bid * 32 + wave * 8 + (lane >> 3), c = lane & 7;
    u32x4 xr[9];
#pragma unroll
    for (int i = 0; i < 9; ++i) xr[i] = ldv<5>(xq + (size_t)row * D_ + (c + 8 * i) * 8);
    float s = 0.f;
#pragma unroll
    for (int i = 0; i < 9; ++i)
#pragma unroll
      for (int e = 0; e < 4; ++e) s += lo16(xr[i][e]) + hi16(xr[i][e]);
    s += __shfl_xor(s, 1); s += __shfl_xor(s, 2); s += __shfl_xor(s, 4);
    const float mean = s * (1.f / 576.f);
    float ss = 0.f;
#pragma unroll
    for (int i = 0; i < 9; ++i)
#pragma unroll
      for (int e = 0; e < 4; ++e) {
        const float a = lo16(xr[i][e]) - mean, b = hi16(xr[i][e]) - mean;
        ss += a * a + b * b;
      }
    ss += __shfl_xor(ss, 1); ss += __shfl_xor(ss, 2); ss += __shfl_xor(ss, 4);
    const float var = bfr(ss * (1.f / 576.f));
    const float sd = sqrtf(bfr(var + 1e-5f));
    if (c == 0) ((unsigned*)(ws + OFF_ST1))[row] = pk2(mean, sd);
  }
}

constexpr int K1_CONV = 144, K1_W = 108;
constexpr int KC_STRIDE = 4416;
constexpr int CH_BYTES = 4 * KC_STRIDE + 4672;
constexpr int K1_XCH = 4 * CH_BYTES;
constexpr int K1_X1 = K1_XCH + 32768;
constexpr int K1_LDS = K1_X1 + 32768;

#define K1_AUX 16
__global__ __launch_bounds__(512, 2) void k1_conv(const u16* __restrict__ xq, const u16* __restrict__ ssm_k, const u16* __restrict__ ssm_D,
                                                  const u16* __restrict__ Wg, const u16* __restrict__ Wu,
                                                  const u16* __restrict__ Wd, char* __restrict__ ws) {
  extern __shared__ __attribute__((aligned(16))) char sm[];
  const int tid = threadIdx.x, lane = tid & 63, wave = __builtin_amdgcn_readfirstlane(tid >> 6);
  const float* mod = (const float*)(ws + OFF_MOD);
  int bid = blockIdx.x;
  if (bid < K1_CONV) {
    const int cg = (bid & 7) * 18 + (bid >> 3);
    const int cl = wave >> 1, w2 = wave & 1;
    const int d = cg * 4 + cl;
    char* chb = sm + cl * CH_BYTES;
    char* ub = chb + 4 * KC_STRIDE;
    const int t128 = tid & 127;
    const char* krow = (const char*)ssm_k + (size_t)d * 4096;
    const char* urow = ws + OFF_UT + (size_t)d * 4096;
    u32x4 ka[2], ua[2];
    u32x2 kb[2];
#pragma unroll
    for (int i = 0; i < 2; ++i) {
      const int c = t128 + 128 * i;
      ka[i] = *(const u32x4*)(krow + c * 16);
      const int cn = c < 255 ? c + 1 : 255;
      kb[i] = *(const u32x2*)(krow + cn * 16);
      if (c == 255) kb[i] = (u32x2){0u, 0u};
      ua[i] = *(const u32x4*)(urow + c * 16);
    }
    const int li = lane & 15, g = lane >> 4;
    const float Dd = bf2f(ssm_D[d]);
    {
      const u32x4 z = {0u, 0u, 0u, 0u};
      if (t128 < 32) *(u32x4*)(ub + t128 * 16) = z;
      else if (t128 < 32 + 12) {
        const int i = t128 - 32;
        *(u32x4*)(chb + (i / 3) * KC_STRIDE + (i % 3) * 16) = z;
      } else if (t128 < 32 + 12 + 16) {
        const int i = t128 - 44;
        *(u32x4*)(chb + (i >> 2) * KC_STRIDE + 64 + 4096 + (i & 3) * 16) = z;
      } else if (t128 < 64) {
        *(u32x4*)(ub + 4608 + (t128 - 60) * 16) = z;
      }
    }
#pragma unroll
    for (int i = 0; i < 2; ++i) {
      const int c = t128 + 128 * i;
      const unsigned w0 = ka[i][0], w1 = ka[i][1], w2_ = ka[i][2], w3 = ka[i][3], w4 = kb[i][0], w5 = kb[i][1];
      const unsigned a0 = __builtin_amdgcn_alignbit(w1, w0, 16), a1 = __builtin_amdgcn_alignbit(w2_, w1, 16),
                     a2 = __builtin_amdgcn_alignbit(w3, w2_, 16), a3 = __builtin_amdgcn_alignbit(w4, w3, 16),
                     a4 = __builtin_amdgcn_alignbit(w5, w4, 16);
      char* p = chb + 64 + c * 16;
      *(u32x4*)(p) = ka[i];
      *(u32x4*)(p + KC_STRIDE) = (u32x4){a0, a1, a2, a3};
      *(u32x4*)(p + 2 * KC_STRIDE) = (u32x4){w1, w2_, w3, w4};
      *(u32x4*)(p + 3 * KC_STRIDE) = (u32x4){a1, a2, a3, a4};
      *(u32x4*)(ub + 512 + c * 16) = ua[i];
      if (c == 0) {
        *(u32x4*)(p - 16) = (u32x4){0u, 0u, 0u, 0u};
        *(u32x4*)(p - 16 + KC_STRIDE) = (u32x4){0u, 0u, 0u, w0 << 16};
        *(u32x4*)(p - 16 + 2 * KC_STRIDE) = (u32x4){0u, 0u, 0u, w0};
        *(u32x4*)(p - 16 + 3 * KC_STRIDE) = (u32x4){0u, 0u, w0 << 16, a0};
      }
    }
    u32x2 xv[4];
    unsigned st[4];
#pragma unroll
    for (int i = 0; i < 4; ++i) {
      const int s = (wave * 8 + (i >> 1) * 4 + g) * 32 + (i & 1) * 16 + li;
      st[i] = ((const unsigned*)(ws + OFF_ST1))[s];
    }
    f32x4 sa = *(const f32x4*)(mod + cg * 4), ca = *(const f32x4*)(mod + 576 + cg * 4);
    f32x4 ga = *(const f32x4*)(mod + 1152 + cg * 4), cb = *(const f32x4*)(mod + 2304 + cg * 4);
    BARRIER();
    const int q = li >> 2, p4 = li & 3;
    const char* abase = chb + ((4 - q) & 3) * KC_STRIDE + 2 * (32 - 8 * g - (q ? 4 : 0) + 4 * p4) - 8;
    const char* bbase = ub + 2 * (256 + 16 * li + 8 * g);
    f32x4 acc[8];
#pragma unroll
    for (int b = 0; b < 8; ++b) acc[b] = (f32x4){0.f, 0.f, 0.f, 0.f};
    u32x2 hp[4], tp[4];
    auto hfun = [&](int i) {
      asm volatile("" : "+v"(st[i]), "+v"(xv[i]));
      const float mu = lo16(st[i]), rs = __builtin_amdgcn_rcpf(hi16(st[i]));
#pragma unroll
      for (int e = 0; e < 2; ++e) {
        float h0 = lo16(xv[i][e]) - mu, h1 = hi16(xv[i][e]) - mu; bfr2(h0, h1);
        h0 *= rs; h1 *= rs; bfr2(h0, h1);
        h0 *= ca[2 * e]; h1 *= ca[2 * e + 1]; bfr2(h0, h1);
        hp[i][e] = pk2(h0 + sa[2 * e], h1 + sa[2 * e + 1]);
      }
      asm volatile("" : "+v"(hp[i]));
    };
    const char* ap = abase + 64 * (1 + w2);
    const char* bp = bbase - 64 * (1 + w2) - 384;
    bf16x8 A[2][8], B[2][8];
#pragma unroll
    for (int i = 0; i < 8; ++i) {
      A[0][i] = __builtin_bit_cast(bf16x8, tr_read2(ap + 512 * i + 8, ap + 512 * i));
      B[0][i] = *(const bf16x8*)(bp + 384 + 512 * i);
    }
    __builtin_amdgcn_sched_barrier(0);
#pragma unroll
    for (int it = 0; it < 4; ++it) {
#pragma unroll
      for (int b = 0; b < 8; ++b) {
        if (it < 3) {
          A[(it + 1) & 1][b] = __builtin_bit_cast(bf16x8, tr_read2(ap + 128 * (it + 1) + 512 * b + 8, ap + 128 * (it + 1) + 512 * b));
          B[(it + 1) & 1][b] = *(const bf16x8*)(bp + 384 - 128 * (it + 1) + 512 * b);
        }
        if ((it == 0 && (b == 2 || b == 6)) || ((it == 1 || it == 2) && b == 2)) {
          const int i = it ? it + 1 : b == 6;
          xv[i] = *(const u32x2*)(xq + (size_t)((wave * 8 + (i >> 1) * 4 + g) * 32 + (i & 1) * 16 + li) * D_ + cg * 4);
        }
        if (it == 1 && b >= 3 && b < 7) tp[b - 3] = *(const u32x2*)(ub + 512 + (256 * (4 * w2 + b - 3) + 16 * li + 4 * g) * 2);
        if (it == 1 && b >= 4) {
          const u32x2 uu = tp[b - 4];
          tp[b - 4] = (u32x2){pk2(lo16(uu[0]) * Dd, hi16(uu[0]) * Dd), pk2(lo16(uu[1]) * Dd, hi16(uu[1]) * Dd)};
          asm volatile("" : "+v"(tp[b - 4]));
        }
        if (it == 2 && b == 6) {
          asm volatile("" : "+v"(ca), "+v"(cb));
#pragma unroll
          for (int e = 0; e < 4; ++e) { ca[e] = bfr(1.f + ca[e]); cb[e] = bfr(1.f + cb[e]); }
          asm volatile("" : "+v"(ca), "+v"(cb));
        }
        if (it == 2 && b == 7) hfun(0);
        if (it == 3 && b >= 5) hfun(b - 4);
#pragma unroll
        for (int qq = 0; qq <= b; ++qq)
          acc[b] = __builtin_amdgcn_mfma_f32_16x16x32_bf16(A[it & 1][qq], B[it & 1][b - qq], acc[b], 0, 0, 0);
        __builtin_amdgcn_sched_barrier(0);
      }
    }
    char* xch = sm + K1_XCH;
    f32x4 fin[4];
    if (w2 == 0) {
#pragma unroll
      for (int i = 0; i < 4; ++i) { *(f32x4*)(xch + wave * 4096 + i * 1024 + lane * 16) = acc[4 + i]; fin[i] = acc[i]; }
    } else {
#pragma unroll
      for (int i = 0; i < 4; ++i) { *(f32x4*)(xch + wave * 4096 + i * 1024 + lane * 16) = acc[i]; fin[i] = acc[4 + i]; }
    }
    {
      const bf16x8 A0 = __builtin_bit_cast(bf16x8, tr_read2(abase + 8, abase));
#pragma unroll
      for (int i = 0; i < 4; ++i) {
        const bf16x8 Bd = *(const bf16x8*)(bbase + 512 * (4 * w2 + i));
        fin[i] = __builtin_amdgcn_mfma_f32_16x16x32_bf16(A0, Bd, fin[i], 0, 0, 0);
      }
    }
    PIN4(fin[0], fin[1], fin[2], fin[3]);
    __syncthreads();
    char* ys = sm + K1_X1;
#pragma unroll
    for (int i = 0; i < 4; ++i) {
      const f32x4 o = *(const f32x4*)(xch + (wave ^ 1) * 4096 + i * 1024 + lane * 16);
      u32x2 y;
#pragma unroll
      for (int e = 0; e < 2; ++e) {
        float y0 = fin[i][2 * e] + o[2 * e], y1 = fin[i][2 * e + 1] + o[2 * e + 1]; bfr2(y0, y1);
        y[e] = pk2(y0 + lo16(tp[i][e]), y1 + hi16(tp[i][e]));
      }
      *(u32x2*)(ys + cl * 4128 + (256 * (4 * w2 + i) + 16 * li + 4 * g) * 2) = y;
    }
    __syncthreads();
    __amdgpu_buffer_rsrc_t xrs = __builtin_amdgcn_make_buffer_rsrc((void*)(ws + OFF_XF), 0, 2 * D_ * S_ * 2, 0x00020000);
#pragma unroll
    for (int j = 0; j < 2; ++j) {
      const int mt = wave * 8 + j * 4 + g;
      const char* yp = ys + q * 4128 + mt * 64 + p4 * 8;
      const u32x4 y = tr_read2(yp, yp + 32);
      u32x4 o1, o2;
#pragma unroll
      for (int e = 0; e < 4; ++e) {
        const unsigned hh = hp[2 * j + (e >> 1)][e & 1], xx = xv[2 * j + (e >> 1)][e & 1];
        const int c = (e & 1) * 2;
        float y0 = lo16(y[e]) * lo16(hh), y1 = hi16(y[e]) * hi16(hh); bfr2(y0, y1);
        y0 *= ga[c]; y1 *= ga[c + 1]; bfr2(y0, y1);
        const unsigned r = pk2(lo16(xx) + y0, hi16(xx) + y1);
        o1[e] = r;
        o2[e] = pk2(lo16(r) * cb[c], hi16(r) * cb[c + 1]);
      }
      const int off = mt * 36864 + cg * 256 + li * 16;
      __builtin_amdgcn_raw_buffer_store_b128(o2, xrs, off, D_ * S_ * 2, K1_AUX);
      __builtin_amdgcn_raw_buffer_store_b128(o1, xrs, off, 0, K1_AUX);
    }
    return;
  }
  bid -= K1_CONV;
  {
    const int cbk = bid % 36, kt = bid / 36;
    const int mat = cbk / 18, cblk = cbk % 18;
    const u16* W = mat ? Wu : Wg;
    const int p = tid & 31, rg = tid >> 5;
    u32x4 w[12];
    float cs[12], sb[12];
#pragma unroll
    for (int i = 0; i < 12; ++i) {
      const int k = kt * 192 + rg + 16 * i;
      w[i] = ldv<4>(W + (size_t)k * F_ + cblk * 256 + p * 8);
    }
    __builtin_amdgcn_sched_barrier(0);
#pragma unroll
    for (int i = 0; i < 12; ++i) {
      const int k = kt * 192 + rg + 16 * i;
      cs[i] = mod[4 * 576 + k];
      sb[i] = mod[3 * 576 + k];
    }
    const int pc = wave * K1_W + bid;
    u32x4 wd[9];
    if (pc < 576) wd_load(wd, Wd, pc, lane);
    __builtin_amdgcn_sched_barrier(0);
    float a1[8], a2[8];
#pragma unroll
    for (int e = 0; e < 8; ++e) { a1[e] = 0.f; a2[e] = 0.f; }
#pragma unroll
    for (int i = 0; i < 12; ++i) {
      const float c1 = bfr(1.f + cs[i]);
#pragma unroll
      for (int e = 0; e < 4; ++e) {
        const float l = lo16(w[i][e]), h = hi16(w[i][e]);
        a1[2 * e] += c1 * l; a1[2 * e + 1] += c1 * h;
        a2[2 * e] += sb[i] * l; a2[2 * e + 1] += sb[i] * h;
      }
    }
    float* red = (float*)(sm + 110592);
#pragma unroll
    for (int e = 0; e < 8; ++e) { a1[e] += __shfl_xor(a1[e], 32); a2[e] += __shfl_xor(a2[e], 32); }
    if (lane < 32) {
      *(f32x4*)(red + wave * 512 + p * 8) = (f32x4){a1[0], a1[1], a1[2], a1[3]};
      *(f32x4*)(red + wave * 512 + p * 8 + 4) = (f32x4){a1[4], a1[5], a1[6], a1[7]};
    } else {
      *(f32x4*)(red + wave * 512 + 256 + p * 8) = (f32x4){a2[0], a2[1], a2[2], a2[3]};
      *(f32x4*)(red + wave * 512 + 256 + p * 8 + 4) = (f32x4){a2[4], a2[5], a2[6], a2[7]};
    }
    BARRIER();
    {
      float r = 0.f;
#pragma unroll
      for (int i = 0; i < 8; ++i) r += red[i * 512 + tid];
      const int which = tid >> 8, col = tid & 255;
      const int ft = cblk * 16 + (col >> 4);
      ((float*)(ws + OFF_CC))[(ft / 9) * 1728 + kt * 576 + which * 288 + (ft % 9) * 32 + mat * 16 + (col & 15)] = r;
    }
    if (pc < 576) wd_store(wd, sm + wave * 9216, ws, pc, lane);
  }
}

constexpr int UNIT = 36864;

__device__ __forceinline__ void dma_unit(const char* gbase  , unsigned voff, unsigned lds_dst  ) {
  unsigned keep;
  asm volatile(
      "s_mov_b32 %0, m0\n\t"
      "s_mov_b32 m0, %3\n\t"
      "s_mov_b32 exec_hi, 0\n\t"
      "global_load_lds_dwordx4 %1, %2\n\t"
      "s_mov_b32 exec_hi, -1\n\t"
      "global_load_lds_dwordx4 %1, %2 offset:512\n\t"
      "global_load_lds_dwordx4 %1, %2 offset:1536\n\t"
      "global_load_lds_dwordx4 %1, %2 offset:2560\n\t"
      "global_load_lds_dwordx4 %1, %2 offset:3584\n\t"
      "s_mov_b32 m0, %0"
      : "=&s"(keep)
      : "v"(voff), "s"(gbase), "s"(lds_dst)
      : "memory");
}
template <int OFF, bool SC1 = false>
__device__ __forceinline__ void ld_frag(u32x4& dst, const char* gbase  , unsigned voff) {
  if constexpr (SC1) asm volatile("global_load_dwordx4 %0, %1, %2 offset:%3 sc1" : "=v"(dst) : "v"(voff), "s"(gbase), "i"(OFF) : "memory");
  else asm volatile("global_load_dwordx4 %0, %1, %2 offset:%3" : "=v"(dst) : "v"(voff), "s"(gbase), "i"(OFF) : "memory");
}
#define H_SC1 1
#define H2_AUX 16
#define WAIT_VM(n) asm volatile("s_waitcnt vmcnt(" #n ")" ::: "memory")

constexpr int K3_UP = 18432;
constexpr int K3_CCRAW = 4 * UNIT;
constexpr int K3_CC = K3_CCRAW + 6912;
constexpr int K3_LDS = K3_CC + 4608;
constexpr int K3_D = 6;

__device__ __forceinline__ void dma_rows(const char* gbase  , const unsigned (&vo)[5], unsigned lds_dst  ) {
  asm volatile(
      "s_mov_b32 m0, %6\n\t"
      "s_mov_b32 exec_hi, 0\n\t"
      "global_load_lds_dwordx4 %0, %5\n\t"
      "s_mov_b32 exec_hi, -1\n\t"
      "global_load_lds_dwordx4 %1, %5 offset:512\n\t"
      "global_load_lds_dwordx4 %2, %5 offset:1536\n\t"
      "global_load_lds_dwordx4 %3, %5 offset:2560\n\t"
      "global_load_lds_dwordx4 %4, %5 offset:3584"
      :
      : "v"(vo[0]), "v"(vo[1]), "v"(vo[2]), "v"(vo[3]), "v"(vo[4]), "s"(gbase), "s"(lds_dst)
      : "memory");
}
#define SB0() __builtin_amdgcn_sched_barrier(0)
#define XBAR() { SB0(); asm volatile("s_barrier" ::: "memory"); SB0(); }

template <int V> struct IC { static constexpr int value = V; };
template <int I0, int I1, class F>
__device__ __forceinline__ void static_for(F&& f) {
  if constexpr (I0 < I1) { f(IC<I0>{}); static_for<I0 + 1, I1>(f); }
}
template <int J>
__device__ __forceinline__ void dma_piece(const char* gbase  , unsigned vo, unsigned lds_dst  ) {
  if constexpr (J == 0)
    asm volatile("s_mov_b32 m0, %2\n\ts_mov_b32 exec_hi, 0\n\tglobal_load_lds_dwordx4 %0, %1\n\ts_mov_b32 exec_hi, -1"
                 :: "v"(vo), "s"(gbase), "s"(lds_dst) : "memory");
  else
    asm volatile("s_mov_b32 m0, %2\n\tglobal_load_lds_dwordx4 %0, %1 offset:%3"
                 :: "v"(vo), "s"(gbase), "s"(lds_dst), "i"(1024 * J - 512) : "memory");
}
template <bool Z>
__device__ __forceinline__ void mma16t(f32x4& c, u32x4 a, u32x4 b) {
  if constexpr (Z) asm volatile("v_mfma_f32_16x16x32_bf16 %0, %1, %2, 0" : "=v"(c) : "v"(a), "v"(b));
  else asm volatile("v_mfma_f32_16x16x32_bf16 %0, %1, %2, %0" : "+v"(c) : "v"(a), "v"(b));
}
#define MFMA_DRAIN() { asm volatile("s_nop 7\n\ts_nop 7" ::: "memory"); SB0(); }
__device__ __forceinline__ float relu(float a) { return __int_as_float(max(__float_as_int(a), 0)); }
__device__ __forceinline__ u32x2 reglu(f32x4 g, f32x4 u) {
  float p[4];
#pragma unroll
  for (int i = 0; i < 4; ++i) asm("v_mul_f32 %0, %1, %2" : "=v"(p[i]) : "v"(relu(g[i])), "v"(u[i]));
  return (u32x2){pk2(p[0], p[1]), pk2(p[2], p[3])};
}

enum { L_NONE = 0, L_X, L_W, L_CC, L_POLL, L_R };
struct K3E {
  int pos, u, k32, tm, off;
  int epi;
  int lk, la, lb;
};
constexpr int K3_N = 342;
struct K3T { K3E e[K3_N]; };
constexpr K3T k3_table() {
  K3T t{};
  int n = 0;
  for (int i = 0; i < 324; ++i) {
    const int u = i / 36, r = i % 36;
    K3E e{};
    e.pos = i; e.u = u; e.epi = -1;
    if (u < 6) {
      e.k32 = 2 * u + r / 18; e.tm = r % 18;
      e.off = (e.tm & 1) * K3_UP + (r / 18) * 9216 + (e.tm >> 1) * 32;
    } else {
      e.k32 = 12 + (r % 12) / 2; e.tm = ((u - 6) * 3 + r / 12) * 2 + (r & 1);
      e.off = (r & 1) * K3_UP + ((r % 12) / 2) * 3072 + (r / 12) * 32;
      if (i > 228 && r % 12 == 2) e.epi = (e.tm >> 1) - 1;
    }
    if (u == 0) {
      if (r < 5) { e.lk = L_W; e.la = 1; e.lb = r; }
      else if (r == 5) e.lk = L_CC;
      else if (r >= 8 && r <= 20 && r % 3 == 2) { e.lk = L_W; e.la = 2; e.lb = (r - 8) / 3; }
      else if (r >= 23 && r <= 32 && r % 3 == 2) { e.lk = L_X; e.la = 8 + (r - 23) / 3; }
    } else if (u == 5) {
      if (r < 4) { e.lk = L_X; e.la = 32 + r; }
      else if (r >= 6 && r <= 18 && r % 3 == 0) { e.lk = L_W; e.la = 7; e.lb = (r - 6) / 3; }
      else if (r == 21) e.lk = L_POLL;
    } else if (u <= 6) {
      if (r <= 12 && r % 3 == 0) { e.lk = L_W; e.la = u + 2; e.lb = r / 3; }
      else if (u < 4 && r >= 15 && r <= 24 && r % 3 == 0) { e.lk = L_X; e.la = 8 + 4 * u + (r - 15) / 3; }
      else if (u == 4 && r >= 15 && r <= 29 && (r & 1)) { e.lk = L_X; e.la = 24 + (r - 15) / 2; }
    } else if (r <= 16 && r % 4 == 0) { e.lk = L_R; e.la = u - 7; e.lb = r / 4; }
    t.e[n++] = e;
    if (u >= 6 && r % 12 == 11) {
      K3E c{};
      c.pos = -1; c.epi = -1;
      c.tm = e.tm - 1; c.off = c.tm * 256; t.e[n++] = c;
      c.tm = e.tm; c.off = c.tm * 256; t.e[n++] = c;
    }
  }
  return t;
}
constexpr K3T K3TAB = k3_table();
constexpr int k3_younger(int J, int lk, int la, int lb) {
  int n = 0;
  for (int j = 0; j < J; ++j) {
    const K3E& e = K3TAB.e[j];
    if (e.epi >= 0) ++n;
    if (e.lk) n += e.lk == L_POLL ? 2 : 1;
    if (e.lk == lk && e.la == la && e.lb == lb) n = 0;
  }
  return n;
}
template <int N> __device__ __forceinline__ void wait_vm() { asm volatile("s_waitcnt vmcnt(%0)" :: "n"(N) : "memory"); }

__device__ __forceinline__ void k3_gemm1(const u16* __restrict__ Wg, const u16* __restrict__ Wu, char* __restrict__ ws, char* sm) {
  const int tid = threadIdx.x, lane = tid & 63;
  const int wave = __builtin_amdgcn_readfirstlane(tid >> 6);
  const int bid = blockIdx.x;
  const int xcd = bid & 7, idx = bid >> 3;
  const int m256 = (xcd >> 2) * 4 + (idx >> 3), sl = (xcd & 3) * 8 + (idx & 7);
  const int mt = m256 * 8 + wave;
  const unsigned voff = lane * 16;
  const unsigned lds0 = (unsigned)(uintptr_t)(LDSP char*)sm;
  const char* wA = (const char*)((wave & 4) ? Wu : Wg) + (size_t)(wave & 3) * 16 * 9216 + sl * 288;
  const char* wB = (const char*)((wave & 4) ? Wu : Wg) + (size_t)(384 + (wave & 3) * 48) * 9216 + sl * 288;
  const unsigned wdst = lds0 + wave * 4608;
  unsigned vo[5];
#pragma unroll
  for (int j = 0; j < 5; ++j) {
    const unsigned off = j ? 1024 * j - 512 : 0, c = off / 16 + lane;
    vo[j] = (c / 18) * 9216 + (c % 18) * 16 - off;
  }
  const char* xsrc = ws + OFF_XS + (size_t)mt * 36864;
  u32x4 X[36];
#define LDX(i) ld_frag<((i) & 3) * 1024>(X[i], xsrc + ((i) >> 2) * 4096, voff)
#define SWP(p, i, q, j) { unsigned a_ = p[i], c_ = q[j]; asm volatile("v_swap_b32 %0, %1" : "+v"(a_), "+v"(c_)); p[i] = a_; q[j] = c_; \
  asm volatile("" : "+v"(p), "+v"(q)); }
#define FIX2(i) { SWP(X[i], 2, X[(i) + 1], 0) SWP(X[i], 3, X[(i) + 1], 1) }
#define FIX4(i) { PIN4(X[i], X[(i) + 1], X[(i) + 2], X[(i) + 3]); FIX2(i) FIX2((i) + 2) \
  asm volatile("s_nop 1" : "+v"(X[i]), "+v"(X[(i) + 1]), "+v"(X[(i) + 2]), "+v"(X[(i) + 3])); }
#define LDX4(i) { LDX(i); LDX((i) + 1); LDX((i) + 2); LDX((i) + 3); }
  dma_rows(wA, vo, wdst + 3 * UNIT);
  LDX4(0)
  XBAR();
  LDX4(4)
  u32x4 sx[9];
  if (wave == 7) {
    const char* sb = ws + OFF_XF + (size_t)(m256 * 8 + (sl >> 2)) * 36864 + (sl & 3) * 64;
    const unsigned so = (lane >> 2) * 256 + (lane & 3) * 16;
#pragma unroll
    for (int i = 0; i < 9; ++i) ld_frag<0>(sx[i], sb + i * 4096, so);
    WAIT_VM(13);
  } else {
    WAIT_VM(4);
  }
  const char* csrc = ws + OFF_CC + (size_t)sl * 6912 + wave * 864;
  const char* w2src = ws + OFF_W2 + (size_t)((xcd & 3) * 4 + (idx & 3)) * 324 * 1024 + wave * 4608;
  const unsigned cdst = lds0 + K3_CCRAW + wave * 864;
  f32x4 acc[36];
  __amdgpu_buffer_rsrc_t hrs = __builtin_amdgcn_make_buffer_rsrc((void*)(ws + OFF_H2 + (size_t)mt * 294912), 0, 294912, 0x00020000);
  const int rw = (lane >> 4) * 4 + ((lane & 15) >> 2);
  lptr bu = (lptr)sm;
  lptr cp = (lptr)sm + K3_CC + (lane & 15) * 16;
  u32x4 A[K3_D];
  unsigned stw[2];
  u32x4 bc[2];
  const char* stp = ws + OFF_ST2 + (size_t)mt * 128;
  auto rd = [&](auto jc, auto hc) -> u32x2 {
    constexpr int H = decltype(hc)::value;
    constexpr K3E E = K3TAB.e[decltype(jc)::value];
    if constexpr (E.pos >= 0) {
      constexpr int RS = E.u < 6 ? 288 : 96;
      if constexpr (E.pos % 36 == 0 && H == 0) { bu = (lptr)sm + ((E.u + 3) & 3) * UNIT + rw * RS + (lane & 3) * 8; asm volatile("" : "+v"(bu)); }
      return __builtin_bit_cast(u32x2, __builtin_amdgcn_ds_read_tr16_b64_v4i16((LDSP s16x4*)(bu + E.off + H * 16 * RS)));
    } else {
      return *(LDSP const u32x2*)(cp + E.off + H * 8);
    }
  };
  auto epi = [&](auto tc) {
    constexpr int T = decltype(tc)::value, c = T * 4;
    const u32x2 h0 = reglu(acc[c], acc[c + 2]), h1 = reglu(acc[c + 1], acc[c + 3]);
    __builtin_amdgcn_raw_buffer_store_b128((u32x4){h0[0], h0[1], h1[0], h1[1]}, hrs, voff, (sl * 9 + T) * 1024, H2_AUX);
    SB0();
  };
#define POLL() asm volatile("global_load_dword %0, %2, %3 sc1\n\tglobal_load_dword %1, %2, %3 offset:64 sc1" \
                            : "=&v"(stw[0]), "=&v"(stw[1]) : "v"((lane & 15) * 4), "s"(stp) : "memory")
  FIX4(0)
  XBAR();
  static_for<0, K3_D>([&](auto jc) {
    const u32x2 n0 = rd(jc, IC<0>{}), n1 = rd(jc, IC<1>{});
    A[decltype(jc)::value] = (u32x4){n0[0], n0[1], n1[0], n1[1]};
  });
  if (wave == 7) {
    WAIT_VM(0);
    PIN4(sx[0], sx[1], sx[2], sx[3]); PIN4(sx[4], sx[5], sx[6], sx[7]); asm volatile("" : "+v"(sx[8]));
    const bf16x2 ones = __builtin_bit_cast(bf16x2, 0x3f803f80u);
    float s1[2] = {0.f, 0.f}, s2[2] = {0.f, 0.f};
#pragma unroll
    for (int i = 0; i < 9; ++i)
#pragma unroll
      for (int e = 0; e < 4; ++e) {
        const unsigned xu = sx[i][e];
        const bf16x2 xx = __builtin_bit_cast(bf16x2, xu);
        s1[e >> 1] = __builtin_amdgcn_fdot2_f32_bf16(xx, ones, s1[e >> 1], false);
        s2[e >> 1] = __builtin_amdgcn_fdot2_f32_bf16(xx, xx, s2[e >> 1], false);
      }
    const bool up = lane & 4;
    float a = (up ? s1[1] : s1[0]) + __shfl_xor(up ? s1[0] : s1[1], 4), b = (up ? s2[1] : s2[0]) + __shfl_xor(up ? s2[0] : s2[1], 4);
#pragma unroll
    for (int o = 8; o < 64; o <<= 1) { a += __shfl_xor(a, o); b += __shfl_xor(b, o); }
    const float mean = a * (1.f / 576.f);
    const float var = fmaxf(b * (1.f / 576.f) - mean * mean, 0.f);
    const unsigned w = pk2(mean, sqrtf(bfr(bfr(var) + 1e-5f)));
    if (lane < 8)
      __hip_atomic_store((GLBP unsigned*)(ws + OFF_ST2) + m256 * 256 + (sl >> 2) * 32 + (sl & 3) * 4 + (lane & 3) + (lane & 4) * 4, w, __ATOMIC_RELAXED, __HIP_MEMORY_SCOPE_AGENT);
  }
  SB0();
  static_for<0, K3_N>([&](auto jc) {
    constexpr int J = decltype(jc)::value;
    constexpr K3E E = K3TAB.e[J];
    constexpr int I = E.pos;
    if constexpr (I >= 0 && I % 36 == 30 && I < 300) {
      wait_vm<k3_younger(J, I == 30 ? L_CC : L_W, I == 30 ? 0 : E.u + 1, I == 30 ? 0 : 4)>();
      XBAR();
    }
    if constexpr (I == 32) {
      if (tid < 288) {
        const float* rp = (const float*)(sm + K3_CCRAW) + tid;
        const float c1 = rp[0] + rp[576] + rp[1152], c2 = rp[288] + rp[864] + rp[1440];
        const float h1 = bfr(c1), r1 = c1 - h1, m1 = bfr(r1), h2 = bfr(c2), r2 = c2 - h2, m2 = bfr(r2);
        *(u32x4*)(sm + K3_CC + tid * 16) = (u32x4){pk2(h1, m1), pk2(r1 - m1, h2), pk2(m2, r2 - m2), 0u};
      }
      SB0();
    }
    if constexpr (I > 0 && (I <= 216 ? I % 36 == 0 : (I == 220 || I == 224))) {
      constexpr int x0 = E.k32 * 2;
      wait_vm<k3_younger(J, L_X, x0 + 3, 0)>();
      FIX4(x0)
      SB0();
    }
    if constexpr (I == 130) {
#pragma unroll
      for (int j = 0; j < 5; ++j) {
        const unsigned off = j ? 1024 * j - 512 : 0, c = off / 16 + lane;
        vo[j] = (c / 6) * 9216 + (c % 6) * 16 - off;
      }
      SB0();
    }
    if constexpr (I == 226) {
      wait_vm<k3_younger(J, L_POLL, 0, 0)>();
      asm volatile("" : "+v"(stw[0]), "+v"(stw[1]));
      for (unsigned spins = 0; __builtin_amdgcn_ballot_w64(stw[0] == 0u || stw[1] == 0u) != 0ull && spins < (1u << 20); ++spins) {
        __builtin_amdgcn_s_sleep(2);
        POLL(); WAIT_VM(0); asm volatile("" : "+v"(stw[0]), "+v"(stw[1]));
      }
#pragma unroll
      for (int m = 0; m < 2; ++m) {
        const unsigned w = lane < 16 ? (stw[m] ^ 0x8000u) : 0u;
        const unsigned nm = w & 0xffffu, sd = w >> 16;
        bc[m] = (u32x4){nm | (nm << 16), w, sd | (sd << 16), 0u};
      }
      asm volatile("" : "+v"(bc[0]), "+v"(bc[1]));
      SB0();
    }
    if constexpr (E.epi >= 0) epi(IC<E.epi>{});
    const u32x4 a = A[J % K3_D];
    constexpr bool Z = I >= 0 && I < 18;
    u32x2 n0, n1;
    if constexpr (I >= 0) mma16t<Z>(acc[2 * E.tm], a, X[E.k32 * 2]); else mma16t<false>(acc[2 * E.tm], a, bc[0]);
    if constexpr (J + K3_D < K3_N) n0 = rd(IC<J + K3_D>{}, IC<0>{});
    SB0();
    if constexpr (I >= 0) mma16t<Z>(acc[2 * E.tm + 1], a, X[E.k32 * 2 + 1]); else mma16t<false>(acc[2 * E.tm + 1], a, bc[1]);
    if constexpr (J + K3_D < K3_N) { n1 = rd(IC<J + K3_D>{}, IC<1>{}); A[J % K3_D] = (u32x4){n0[0], n0[1], n1[0], n1[1]}; }
    if constexpr (E.lk == L_X) LDX(E.la);
    if constexpr (E.lk == L_W)
      dma_piece<E.lb>(E.la < 6 ? wA + (size_t)E.la * 64 * 9216 : wB + (E.la - 6) * 96, vo[E.lb], wdst + ((E.la + 3) & 3) * UNIT);
    if constexpr (E.lk == L_R) dma_piece<E.lb>(w2src + E.la * UNIT, voff, wdst + E.la * UNIT);
    if constexpr (E.lk == L_CC)
      asm volatile("s_mov_b32 m0, %2\n\ts_mov_b32 exec_hi, 0x3fffff\n\tglobal_load_lds_dwordx4 %0, %1\n\ts_mov_b32 exec_hi, -1"
                   :: "v"(voff), "s"(csrc), "s"(cdst) : "memory");
    if constexpr (E.lk == L_POLL) POLL();
    SB0();
  });
  MFMA_DRAIN();
  epi(IC<8>{});
}

constexpr int K4_LDS = 4 * UNIT + 16384;
constexpr int K4_N = 162, K4_D = 6;
#define K4_PRE3 1
struct K4E { int u, q, hb, b, off, lk, la, lb; };
struct K4T { K4E e[K4_N]; };
constexpr K4T k4_table() {
  K4T t{};
  for (int i = 0; i < K4_N; ++i) {
    const int u = i / 18, r = i % 18;
    K4E e{};
    e.u = u;
    e.q = u < 6 ? r % 9 : (u - 6) * 3 + r / 6;
    e.hb = u < 6 ? 2 * u + r / 9 : 12 + r % 6;
    e.b = e.q == 6 ? 2 : (e.q < 6 ? e.q & 1 : e.q - 7);
    const int pb = e.q == 6 ? 6 : e.q - e.b;
    e.off = u < 6 ? ((r / 9) * 18 + pb) * 1024 : ((u == 8 ? pb - 6 : pb) * 12 + 2 * (r % 6)) * 1024;
    if (u == 0) {
      if (r % 3 != 2) { e.lk = L_X; e.la = 8 + r - r / 3; }
      else if (!K4_PRE3 && r < 15) { e.lk = L_W; e.la = 3; e.lb = r / 3; }
    } else if (u <= 5) {
      if (r <= 8 && !(r & 1)) { e.lk = L_W; e.la = u + 3; e.lb = r / 2; }
      else if (u <= 4 && r >= 10 && !(r & 1)) { e.lk = L_X; e.la = 16 + 4 * u + (r - 10) / 2; }
      else if (u == 5 && r >= 10 && r < 17) { e.lk = L_R; e.la = r - 10; }
    }
    t.e[i] = e;
  }
  return t;
}
constexpr K4T K4TAB = k4_table();
constexpr int k4_send(int i) { return i == 122 ? 0 : i == 134 ? 1 : i == 146 ? 2 : -1; }
constexpr int k4_younger(int J, int lk, int la, int lb, int pre) {
  int n = pre;
  for (int j = 0; j < J; ++j) {
    const K4E& e = K4TAB.e[j];
    if (k4_send(j) >= 0) ++n;
    if (e.lk) ++n;
    if (e.lk == lk && e.la == la && e.lb == lb) n = 0;
  }
  return n;
}
constexpr int k4_xoff(int j) { return j < 2 ? UNIT + j * 16384 : j == 2 ? 4 * UNIT : 2 * UNIT + (j - 3) * 16384; }
__device__ __forceinline__ f32x4 add4(f32x4 a, f32x4 b) {
  f32x4 r;
#pragma unroll
  for (int i = 0; i < 4; ++i) asm("v_add_f32 %0, %1, %2" : "=v"(r[i]) : "v"(a[i]), "v"(b[i]));
  return r;
}

__global__ __launch_bounds__(512, 2) void k34_mlp(const u16* __restrict__ Wg, const u16* __restrict__ Wu, char* __restrict__ ws,
                                                  u16* __restrict__ out) {
  extern __shared__ __attribute__((aligned(16))) char sm[];
  int wave = __builtin_amdgcn_readfirstlane(threadIdx.x >> 6), bid = blockIdx.x;
  k3_gemm1(Wg, Wu, ws, sm);
  int lane;
  asm volatile("v_mbcnt_lo_u32_b32 %0, -1, 0\n\tv_mbcnt_hi_u32_b32 %0, -1, %0\n\ts_waitcnt vmcnt(0)" : "=v"(lane), "+s"(wave), "+s"(bid), "+s"(ws) :: "memory");
  const int tid = wave * 64 + lane;
  GLBP unsigned* grp = (GLBP unsigned*)(ws + OFF_FLG + ((bid >> 6) * 8 + (bid & 7)) * 512);
  XBAR();
  const int wm = wave & 3, wk = wave >> 2;
  const int xcd = bid & 7, idx = bid >> 3;
  const int kb = xcd & 3;
  const int m128 = (xcd >> 2) * 8 + (idx >> 2), nt = idx & 3;
  const unsigned voff = lane * 16;
  const unsigned lds0 = (unsigned)(uintptr_t)(LDSP char*)sm;
  const char* wsrc = ws + OFF_W2 + (size_t)(kb * 4 + nt) * 324 * 1024 + wave * 4608;
  const unsigned wdst = lds0 + wave * 4608;
  const char* h0 = ws + OFF_H2 + ((size_t)(m128 * 4 + wm) * 288 + (kb * 36 + wk) * 2) * 1024;
  u32x4 H[36];
#define LDH(i) ld_frag<((i) & 1) * 1024, H_SC1>(H[i], h0 + ((i) >> 1) * 4096, voff)
  if (tid == 0) __hip_atomic_fetch_add(grp, 1u, __ATOMIC_RELAXED, __HIP_MEMORY_SCOPE_AGENT);
  dma_unit(wsrc + 2 * UNIT, voff, wdst + 2 * UNIT);
  dma_unit(wsrc + 3 * UNIT, voff, wdst + 3 * UNIT);
  if (wave == 0) {
    for (unsigned spins = 0; spins < (1u << 22); ++spins) {
      if ((unsigned)__builtin_amdgcn_readfirstlane(__hip_atomic_load(grp, __ATOMIC_RELAXED, __HIP_MEMORY_SCOPE_AGENT)) >= 8u) break;
      __builtin_amdgcn_s_sleep(1);
    }
  }
  XBAR();
  LDH(0); LDH(1); LDH(2); LDH(3);
  LDH(4); LDH(5); LDH(6); LDH(7);
  f32x4 acc[18];
  u32x4 A[K4_D];
  lptr bu[3];
  LDSP char* xw = (LDSP char*)sm + wave * 2048 + lane * 16;
  lptr xr_ = (lptr)sm + (wave ^ 4) * 2048 + lane * 16;
  const int tile = m128 * 4 + nt;
  __amdgpu_buffer_rsrc_t prs = __builtin_amdgcn_make_buffer_rsrc((void*)(ws + OFF_PART + (size_t)tile * PART_TILE), 0, PART_TILE, 0x00020000);
  f32x4 oth[2], f8;
  const int r0 = m128 * 128 + wm * 32, dA = nt * 9 + 2 * kb + wk, d8 = nt * 9 + 8;
  const char* xfA = ws + OFF_XF + (size_t)(r0 >> 5) * 36864 + dA * 1024;
  const char* xf8 = ws + OFF_XF + (size_t)(r0 >> 5) * 36864 + d8 * 1024 + wk * 8;
  const char* gbp = ws + OFF_MOD + 5 * 576 * 4;
  const char* stp = ws + OFF_ST2 + r0 * 4;
  u32x2 xres[3];
  u32x4 gbv[2];
  unsigned stw[2];
  auto rd = [&](auto jc) {
    constexpr int J = decltype(jc)::value;
    constexpr K4E E = K4TAB.e[J];
    if constexpr (J % 18 == 0 && E.u != 7) {
      constexpr int ST = E.u < 6 ? 1024 : 12288;
      bu[2] = (lptr)sm + (E.u & 3) * UNIT + wk * (E.u < 6 ? 9216 : 1024) + lane * 16;
      bu[1] = bu[2] + wk * ST; bu[0] = bu[2] + (1 - wk) * ST;
      asm volatile("" : "+v"(bu[0]), "+v"(bu[1]), "+v"(bu[2]));
    }
    return *(LDSP const u32x4*)(bu[E.b] + E.off);
  };
  SB0();
  static_for<0, K4_D>([&](auto jc) { A[decltype(jc)::value] = rd(jc); });
  SB0();
  static_for<0, K4_N>([&](auto jc) {
    constexpr int J = decltype(jc)::value;
    constexpr K4E E = K4TAB.e[J];
    constexpr int R = J - 108;
    if constexpr (J % 18 == 12 && J < 108) {
      wait_vm<k4_younger(J, L_W, E.u + 1, 4, 8)>();
      asm volatile("s_waitcnt lgkmcnt(0)" ::: "memory");
      XBAR();
    }
    if constexpr (R == 3) { wait_vm<k4_younger(J, L_W, 7, 4, 0)>(); XBAR(); }
    if constexpr (R == 7 || R == 19 || R == 31 || R == 49) {
      constexpr int j = R == 49 ? 4 : R / 12, q = E.q - 1;
      *(LDSP f32x4*)(xw + k4_xoff(j)) = acc[2 * q];
      *(LDSP f32x4*)(xw + k4_xoff(j) + 1024) = acc[2 * q + 1];
      SB0();
    }
    if constexpr (R == 43) {
      if (wk) { *(LDSP f32x4*)(xw + k4_xoff(3)) = acc[12]; f8 = acc[13]; } else { *(LDSP f32x4*)(xw + k4_xoff(3)) = acc[13]; f8 = acc[12]; }
      SB0();
    }
    if constexpr (R == 9 || R == 21 || R == 33 || R == 45 || R == 51) {
      if constexpr (R == 21) wait_vm<k4_younger(J, L_W, 8, 4, 0)>();
      asm volatile("s_waitcnt lgkmcnt(%0)" :: "n"(R < 48 ? 2 : 0) : "memory");
      XBAR();
      constexpr int j = R == 51 ? 4 : R / 12;
      oth[0] = *(LDSP const f32x4*)(xr_ + k4_xoff(j));
      if constexpr (j != 3) oth[1] = *(LDSP const f32x4*)(xr_ + k4_xoff(j) + 1024);
      SB0();
    }
    if constexpr (k4_send(J) >= 0) {
      constexpr int j = k4_send(J);
      const f32x4 s0 = add4(acc[4 * j + 2], oth[0]), s1 = add4(acc[4 * j + 3], oth[1]);
      __builtin_amdgcn_raw_buffer_store_b128((u32x4){pk2(s0[0], s0[1]), pk2(s0[2], s0[3]), pk2(s1[0], s1[1]), pk2(s1[2], s1[3])}, prs, voff,
                                             ((((kb + j + 1) & 3) * 4 + kb) * 8 + wave) * 1024, 16);
      SB0();
    }
    if constexpr (R == 47) {
      f8 = add4(f8, oth[0]);
      if (wm != kb)
        __builtin_amdgcn_raw_buffer_store_b64((u32x2){pk2(f8[0], f8[1]), pk2(f8[2], f8[3])}, prs, lane * 8, 131072 + ((wm * 4 + kb) * 2 + wk) * 512, 16);
      SB0();
    }
    constexpr bool first = E.u < 6 ? E.q == 0 : J < 114;
    if constexpr (first) {
      constexpr int hb = E.hb;
      wait_vm<k4_younger(J, L_X, 2 * hb + 1, 0, hb < 4 ? 6 - 2 * hb : 0)>();
      asm volatile("" : "+v"(H[2 * hb]), "+v"(H[2 * hb + 1]));
      const u32x4 l0 = H[2 * hb], l1 = H[2 * hb + 1];
      H[2 * hb] = (u32x4){l0[0], l0[1], l1[0], l1[1]};
      H[2 * hb + 1] = (u32x4){l0[2], l0[3], l1[2], l1[3]};
      asm volatile("s_nop 1" : "+v"(H[2 * hb]), "+v"(H[2 * hb + 1]));
      SB0();
    }
    const u32x4 a = A[J % K4_D];
    mma16t<(J < 9)>(acc[2 * E.q], a, H[2 * E.hb]);
    if constexpr (J + K4_D < K4_N) A[J % K4_D] = rd(IC<J + K4_D>{});
    SB0();
    mma16t<(J < 9)>(acc[2 * E.q + 1], a, H[2 * E.hb + 1]);
    if constexpr (E.lk == L_X) LDH(E.la);
    if constexpr (E.lk == L_W) dma_piece<E.lb>(wsrc + E.la * UNIT, voff, wdst + (E.la & 3) * UNIT);
    if constexpr (E.lk == L_R) {
      if constexpr (E.la < 3)
        asm volatile("global_load_dwordx2 %0, %1, %2" : "=v"(xres[E.la]) : "v"(voff), "s"(E.la == 2 ? xf8 : xfA + E.la * 8) : "memory");
      else if constexpr (E.la < 5)
        asm volatile("global_load_dwordx4 %0, %1, %2" : "=v"(gbv[E.la - 3]) : "v"(lane & 48), "s"(gbp + (E.la == 3 ? dA : d8) * 64) : "memory");
      else
        asm volatile("global_load_dword %0, %1, %2 sc1" : "=v"(stw[E.la - 5]) : "v"((lane & 15) * 4), "s"(stp + (E.la - 5) * 64) : "memory");
    }
    SB0();
  });
  WAIT_VM(0);
  asm volatile("" : "+v"(xres[0]), "+v"(xres[1]), "+v"(xres[2]), "+v"(gbv[0]), "+v"(gbv[1]), "+v"(stw[0]), "+v"(stw[1]));
  XBAR();
  GLBP unsigned* cnt = (GLBP unsigned*)(ws + OFF_CNT + tile * 128);
  if (tid == 0) __hip_atomic_fetch_add(cnt, 1u, __ATOMIC_RELAXED, __HIP_MEMORY_SCOPE_AGENT);
  MFMA_DRAIN();
  f32x4 fin[3] = {add4(acc[16], oth[0]), add4(acc[17], oth[1]), f8};
  float rs2[3];
#pragma unroll
  for (int i = 0; i < 2; ++i) { const float r = 1.f / hi16(stw[i]); rs2[i] = r * r; }
  rs2[2] = wk ? rs2[1] : rs2[0];
  if (wave == 0) {
    for (unsigned spins = 0; spins < (1u << 22); ++spins) {
      if ((unsigned)__builtin_amdgcn_readfirstlane(__hip_atomic_load(cnt, __ATOMIC_RELAXED, __HIP_MEMORY_SCOPE_AGENT)) >= 4u) break;
      __builtin_amdgcn_s_sleep(1);
    }
  }
  BARRIER();
  asm volatile(".Lk34_tail:");
  __amdgpu_buffer_rsrc_t ors = __builtin_amdgcn_make_buffer_rsrc((void*)out, 0, S_ * D_ * 2, 0x00020000);
  u32x4 pp[3];
  u32x2 p8[3];
#pragma unroll
  for (int j = 0; j < 3; ++j) {
    const int src = (kb + 1 + j) & 3;
    pp[j] = __builtin_amdgcn_raw_buffer_load_b128(prs, voff, ((kb * 4 + src) * 8 + wave) * 1024, 16);
    p8[j] = __builtin_amdgcn_raw_buffer_load_b64(prs, lane * 8, 131072 + ((kb * 4 + src) * 2 + wk) * 512, 16);
  }
#pragma unroll
  for (int i = 0; i < 3; ++i) {
    if (i == 2 && wm != kb) break;
    u32x2 o;
#pragma unroll
    for (int e = 0; e < 2; ++e) {
      const unsigned q0 = i < 2 ? pp[0][2 * i + e] : p8[0][e], q1 = i < 2 ? pp[1][2 * i + e] : p8[1][e], q2 = i < 2 ? pp[2][2 * i + e] : p8[2][e];
      float a = ((fin[i][2 * e] + lo16(q0)) + (lo16(q1) + lo16(q2))) * rs2[i];
      float b = ((fin[i][2 * e + 1] + hi16(q0)) + (hi16(q1) + hi16(q2))) * rs2[i];
      bfr2(a, b);
      const unsigned g0 = gbv[i >> 1][2 * e], g1 = gbv[i >> 1][2 * e + 1];
      a *= __uint_as_float(g0); b *= __uint_as_float(g1); bfr2(a, b);
      o[e] = pk2(lo16(xres[i][e]) + a, hi16(xres[i][e]) + b);
    }
    const int m = r0 + (i < 2 ? i : wk) * 16 + (lane & 15);
    __builtin_amdgcn_raw_buffer_store_b64(o, ors, (m * D_ + (i < 2 ? dA : d8) * 16 + (lane >> 4) * 4) * 2, 0, 16);
  }
}

extern "C" void kernel_launch(void* const* d_in, const int* in_sizes, int n_in, void* d_out, int out_size,
                              void* d_ws, size_t ws_size, hipStream_t stream) {
  const u16* A_q = (const u16*)d_in[0];
  const u16* A_k = (const u16*)d_in[1];
  const u16* A_v = (const u16*)d_in[2];
  const u16* A_sh = (const u16*)d_in[3];
  const u16* A_sk = (const u16*)d_in[4];
  const u16* A_D = (const u16*)d_in[5];
  const u16* Wg = (const u16*)d_in[7];
  const u16* Wu = (const u16*)d_in[8];
  const u16* Wd = (const u16*)d_in[9];
  const u16* cv = (const u16*)d_in[10];
  const u16* wm = (const u16*)d_in[11];
  char* ws = (char*)d_ws;
  (void)hipFuncSetAttribute((const void*)k1_conv, hipFuncAttributeMaxDynamicSharedMemorySize, K1_LDS);
  (void)hipFuncSetAttribute((const void*)k34_mlp, hipFuncAttributeMaxDynamicSharedMemorySize, K4_LDS);
  k0_prep<<<K0_GRID, 256, 0, stream>>>(A_q, A_k, A_v, A_sh, cv, wm, A_sk, ws);
  k1_conv<<<K1_CONV + K1_W, 512, K1_LDS, stream>>>(A_q, A_sk, A_D, Wg, Wu, Wd, ws);
  k34_mlp<<<256, 512, K4_LDS, stream>>>(Wg, Wu, ws, (u16*)d_out);
}
```
